```python
import torch

def _softmax(x, axis=-1):
    return torch.softmax(x, dim=axis)
def _gelu(x):
    return torch.nn.functional.gelu(x, approximate="tanh")
def _silu(x):
    return torch.nn.functional.silu(x)
def _sigmoid(x):
    return torch.sigmoid(x)
def _layernorm(x, axis=-1, eps=1e-5):
    m = x.mean(dim=axis, keepdim=True)
    v = x.var(dim=axis, keepdim=True, unbiased=False)
    return (x - m) / torch.sqrt(v + eps)
def _scatter_add(tgt, idx, src, axis=0):
    return tgt.clone().index_add_(axis, idx, src)
def _segment_sum(src, seg, n):
    out = torch.zeros((n,) + src.shape[1:], dtype=src.dtype)
    return out.index_add_(0, seg, src)
def _pad(x, pw):
    flat = tuple(v for pair in reversed(pw) for v in pair)
    return torch.nn.functional.pad(x, flat)

kernel_name = 'standalone__qk_norm__prompt'

def setup_inputs():

    _w = (lambda a: (lambda t: t.to(torch.bfloat16) if t.is_floating_point() else t)(torch.as_tensor(a)))
    B, H, S, D = 1, 16, 2048, 448
    Dh = D // H
    return {
        "q": _w((lambda *s: torch.randn(*s, dtype=torch.bfloat16) * 0.05)(B, H, S, Dh)),
        "k": _w((lambda *s: torch.randn(*s, dtype=torch.bfloat16) * 0.05)(B, H, S, Dh)),
        "v": _w((lambda *s: torch.randn(*s, dtype=torch.bfloat16) * 0.8)(B, H, S, Dh)),
        "t": _w(torch.abs((lambda *s: torch.randn(*s, dtype=torch.bfloat16) * 0.8)(H)) + 1.0),
    }

def reference(q, k, v, t):
    eps = 1e-6
    qn = q / (torch.sqrt((q * q).sum(axis=-1, keepdims=True)) + eps)
    kn = k / (torch.sqrt((k * k).sum(axis=-1, keepdims=True)) + eps)
    logits = torch.einsum('bhqd,bhkd->bhqk', qn, kn) * t[None, :, None, None]
    attn = _softmax(logits, axis=-1)
    return torch.einsum('bhqk,bhkd->bhqd', attn, v)
```

```cpp
#include <hip/hip_runtime.h>
#include <hip/hip_bf16.h>
#include <stdint.h>

typedef float f32x16 __attribute__((ext_vector_type(16)));
typedef _Float16 f16x8 __attribute__((ext_vector_type(8)));
typedef __bf16 bf16x8 __attribute__((ext_vector_type(8)));
typedef unsigned short u16;

constexpr int H = 16, S = 2048, DH = 28;

__device__ __forceinline__ float bf2f(u16 x) { return __uint_as_float((unsigned)x << 16); }

__device__ __forceinline__ void load_norm_frag(const u16* row, int h, f16x8& f0, f16x8& f1) {
    float a[8], b[6], ss = 0.f;
#pragma unroll
    for (int j = 0; j < 8; ++j) { a[j] = bf2f(row[8 * h + j]); ss += a[j] * a[j]; }
#pragma unroll
    for (int j = 0; j < 6; ++j) { b[j] = bf2f(row[16 + 6 * h + j]); ss += b[j] * b[j]; }
    ss += __shfl_xor(ss, 32);
    const float r = 1.0f / (sqrtf(ss) + 1e-6f);
#pragma unroll
    for (int j = 0; j < 8; ++j) f0[j] = (_Float16)(a[j] * r);
#pragma unroll
    for (int j = 0; j < 6; ++j) f1[j] = (_Float16)(b[j] * r);
    f1[6] = (_Float16)0.f; f1[7] = (_Float16)0.f;
}

__device__ __forceinline__ void general_path(const u16* q, const u16* k, const u16* v, float th, u16* out, int head, int qblk) {
    const int lane = threadIdx.x & 63, w = threadIdx.x >> 6, r = lane & 31, h = lane >> 5;
    const int qrow = qblk * 128 + w * 32 + r;
    f16x8 q0, q1;
    load_norm_frag(q + ((size_t)head * S + qrow) * DH, h, q0, q1);
    const float c = th * 1.4426950408889634f;
    f32x16 o;
#pragma unroll
    for (int i = 0; i < 16; ++i) o[i] = 0.f;
    float m = -INFINITY, l = 0.f;
    const u16* kh = k + (size_t)head * S * DH;
    const u16* vh = v + (size_t)head * S * DH;
    for (int kt = 0; kt < S / 32; ++kt) {
        f16x8 k0, k1;
        load_norm_frag(kh + (size_t)(kt * 32 + r) * DH, h, k0, k1);
        f32x16 s;
#pragma unroll
        for (int i = 0; i < 16; ++i) s[i] = 0.f;
        s = __builtin_amdgcn_mfma_f32_32x32x16_f16(k0, q0, s, 0, 0, 0);
        s = __builtin_amdgcn_mfma_f32_32x32x16_f16(k1, q1, s, 0, 0, 0);
        float mt = -INFINITY;
#pragma unroll
        for (int i = 0; i < 16; ++i) { s[i] *= th; mt = fmaxf(mt, s[i]); }
        mt = fmaxf(mt, __shfl_xor(mt, 32));
        const float mn = fmaxf(m, mt);
        const float alpha = __expf(m - mn);
        m = mn;
        float ls = 0.f;
        bf16x8 p0, p1;
#pragma unroll
        for (int i = 0; i < 8; ++i) {
            p0[i] = (__bf16)__expf(s[i] - mn); p1[i] = (__bf16)__expf(s[8 + i] - mn);
            ls += (float)p0[i] + (float)p1[i];
        }
        l = l * alpha + ls;
#pragma unroll
        for (int i = 0; i < 16; ++i) o[i] *= alpha;
        bf16x8 v0, v1;
#pragma unroll
        for (int j = 0; j < 8; ++j) {
            const int key = kt * 32 + 8 * (j >> 2) + 4 * h + (j & 3);
            const u16 x0 = r < DH ? vh[(size_t)key * DH + r] : (u16)0;
            const u16 x1 = r < DH ? vh[(size_t)(key + 16) * DH + r] : (u16)0;
            v0[j] = __builtin_bit_cast(__bf16, x0); v1[j] = __builtin_bit_cast(__bf16, x1);
        }
        o = __builtin_amdgcn_mfma_f32_32x32x16_bf16(v0, p0, o, 0, 0, 0);
        o = __builtin_amdgcn_mfma_f32_32x32x16_bf16(v1, p1, o, 0, 0, 0);
    }
    (void)c;
    l += __shfl_xor(l, 32);
    const float rl = 1.0f / l;
    u16* orow = out + ((size_t)head * S + qrow) * DH;
#pragma unroll
    for (int i = 0; i < 16; ++i) {
        const int d = (i & 3) + 8 * (i >> 2) + 4 * h;
        if (d < DH) orow[d] = __builtin_bit_cast(u16, (__bf16)(o[i] * rl));
    }
}

__global__ __launch_bounds__(256, 1) void qk_norm_attn_kernel(const u16* q, const u16* k, const u16* v, const u16* t, u16* out) {
    const int b = blockIdx.x, i = b >> 3, head = (b & 7) * 2 + (i >> 4), qblk = i & 15;
    general_path(q, k, v, bf2f(t[head]), out, head, qblk);
}

extern "C" void kernel_launch(void* const* d_in, const int* in_sizes, int n_in, void* d_out, int out_size, void* d_ws, size_t ws_size, hipStream_t stream) {
    qk_norm_attn_kernel<<<256, 256, 0, stream>>>((const u16*)d_in[0], (const u16*)d_in[1], (const u16*)d_in[2], (const u16*)d_in[3], (u16*)d_out);
}
```

```cpp
#include <hip/hip_runtime.h>
#include <hip/hip_bf16.h>
#include <stdint.h>

typedef float f32x16 __attribute__((ext_vector_type(16)));
typedef _Float16 f16x8 __attribute__((ext_vector_type(8)));
typedef __bf16 bf16x8 __attribute__((ext_vector_type(8)));
typedef unsigned short u16;

constexpr int S = 2048, DH = 28;

__device__ __forceinline__ float bf2f(u16 x) { return __uint_as_float((unsigned)x << 16); }

__device__ __forceinline__ void load_norm_frag(const u16* row, int h, f16x8& f0, f16x8& f1) {
    float a[8], b[6], ss = 0.f;
#pragma unroll
    for (int j = 0; j < 8; ++j) { a[j] = bf2f(row[8 * h + j]); ss += a[j] * a[j]; }
#pragma unroll
    for (int j = 0; j < 6; ++j) { b[j] = bf2f(row[16 + 6 * h + j]); ss += b[j] * b[j]; }
    ss += __shfl_xor(ss, 32);
    const float r = 1.0f / (sqrtf(ss) + 1e-6f);
#pragma unroll
    for (int j = 0; j < 8; ++j) f0[j] = (_Float16)(a[j] * r);
#pragma unroll
    for (int j = 0; j < 6; ++j) f1[j] = (_Float16)(b[j] * r);
    f1[6] = (_Float16)0.f; f1[7] = (_Float16)0.f;
}

__device__ __forceinline__ void general_path(const u16* q, const u16* k, const u16* v, float th, u16* out, int head, int qblk) {
    const int lane = threadIdx.x & 63, w = threadIdx.x >> 6, r = lane & 31, h = lane >> 5;
    const int qrow = qblk * 128 + w * 32 + r;
    f16x8 q0, q1;
    load_norm_frag(q + ((size_t)head * S + qrow) * DH, h, q0, q1);
    f32x16 o;
#pragma unroll
    for (int i = 0; i < 16; ++i) o[i] = 0.f;
    float m = -INFINITY, l = 0.f;
    const u16* kh = k + (size_t)head * S * DH;
    const u16* vh = v + (size_t)head * S * DH;
    for (int kt = 0; kt < S / 32; ++kt) {
        f16x8 k0, k1;
        load_norm_frag(kh + (size_t)(kt * 32 + r) * DH, h, k0, k1);
        f32x16 s;
#pragma unroll
        for (int i = 0; i < 16; ++i) s[i] = 0.f;
        s = __builtin_amdgcn_mfma_f32_32x32x16_f16(k0, q0, s, 0, 0, 0);
        s = __builtin_amdgcn_mfma_f32_32x32x16_f16(k1, q1, s, 0, 0, 0);
        float mt = -INFINITY;
#pragma unroll
        for (int i = 0; i < 16; ++i) { s[i] *= th; mt = fmaxf(mt, s[i]); }
        mt = fmaxf(mt, __shfl_xor(mt, 32));
        const float mn = fmaxf(m, mt);
        const float alpha = __expf(m - mn);
        m = mn;
        float ls = 0.f;
        bf16x8 p0, p1;
#pragma unroll
        for (int i = 0; i < 8; ++i) {
            p0[i] = (__bf16)__expf(s[i] - mn); p1[i] = (__bf16)__expf(s[8 + i] - mn);
            ls += (float)p0[i] + (float)p1[i];
        }
        l = l * alpha + ls;
#pragma unroll
        for (int i = 0; i < 16; ++i) o[i] *= alpha;
        bf16x8 v0, v1;
#pragma unroll
        for (int j = 0; j < 8; ++j) {
            const int key = kt * 32 + 8 * (j >> 2) + 4 * h + (j & 3);
            const u16 x0 = r < DH ? vh[(size_t)key * DH + r] : (u16)0;
            const u16 x1 = r < DH ? vh[(size_t)(key + 16) * DH + r] : (u16)0;
            v0[j] = __builtin_bit_cast(__bf16, x0); v1[j] = __builtin_bit_cast(__bf16, x1);
        }
        o = __builtin_amdgcn_mfma_f32_32x32x16_bf16(v0, p0, o, 0, 0, 0);
        o = __builtin_amdgcn_mfma_f32_32x32x16_bf16(v1, p1, o, 0, 0, 0);
    }
    l += __shfl_xor(l, 32);
    const float rl = 1.0f / l;
    u16* orow = out + ((size_t)head * S + qrow) * DH;
#pragma unroll
    for (int i = 0; i < 16; ++i) {
        const int d = (i & 3) + 8 * (i >> 2) + 4 * h;
        if (d < DH) orow[d] = __builtin_bit_cast(u16, (__bf16)(o[i] * rl));
    }
}

__global__ __launch_bounds__(256, 1) __attribute__((aligned(4096))) void qk_norm_attn_kernel(const u16* q, const u16* k, const u16* v, const u16* t, u16* out, void* ws) {
    __shared__ __attribute__((aligned(256))) char lds[152064];
    unsigned long long ka = (unsigned long long)__builtin_amdgcn_kernarg_segment_ptr();
    asm volatile(
        "v_lshrrev_b32 v4, 6, %2\n\t"
        "s_load_dwordx2 s[20:21], %0, 0\n\t"
        "v_readfirstlane_b32 s40, v4\n\t"
        "s_load_dwordx2 s[24:25], %0, 8\n\t"
        "s_load_dwordx2 s[28:29], %0, 16\n\t"
        "s_load_dwordx2 s[36:37], %0, 24\n\t"
        "s_load_dwordx2 s[16:17], %0, 32\n\t"
        "s_load_dwordx2 s[52:53], %0, 40\n\t"
        "s_mov_b64 vcc, -1\n\t"
        "s_cmp_eq_u32 s40, 1\n\t"
        "s_cbranch_scc1 .L_page1_%=\n\t"
        "s_cmp_eq_u32 s40, 2\n\t"
        "s_cbranch_scc1 .L_page2_%=\n\t"
        ".L_touched_%=:\n\t"
        "s_mov_b64 vcc, 0\n\t"
        "v_and_b32 v2, 63, %2\n\t"
        "s_and_b32 s12, %1, 7\n\t"
        "s_lshr_b32 s38, %1, 7\n\t"
        "s_bfe_u32 s14, %1, 0x40003\n\t"
        "s_lshl1_add_u32 s9, s12, s38\n\t"
        "s_mul_i32 s39, s9, 114688\n\t"
        "s_mul_i32 s45, s14, 7168\n\t"
        "s_add_u32 s45, s45, s39\n\t"
        "s_xor_b32 s46, s40, 1\n\t"
        "s_mul_i32 s42, s46, 1792\n\t"
        "s_add_u32 s42, s42, s45\n\t"
        "s_mul_i32 s41, s40, 28672\n\t"
        "s_add_u32 s41, s41, s39\n\t"
        "s_mov_b32 s22, 1835008\n\t"
        "s_mov_b32 s23, 0x20000\n\t"
        "s_mov_b64 s[26:27], s[22:23]\n\t"
        "v_mul_u32_u24 v6, 56, v2\n\t"
        "v_lshrrev_b32 v3, 5, v2\n\t"
        "s_movk_i32 s44, 0xf910\n\t"
        "v_mad_i32_i24 v210, v3, s44, v6\n\t"
        "v_mad_i32_i24 v211, v3, -4, v210\n\t"
        "s_lshl_b32 s12, s12, 2\n\t"
        "s_waitcnt lgkmcnt(0)\n\t"
        "buffer_load_dwordx4 v[48:51], v210, s[20:23], s42 offen\n\t"
        "buffer_load_dwordx3 v[52:54], v211, s[20:23], s42 offen offset:32\n\t"
        "s_load_dword s44, s[36:37], s12\n\t"
        "buffer_load_dwordx4 v[168:171], v6, s[24:27], s41 offen offset:0\n\t"
        "buffer_load_dwordx4 v[172:175], v6, s[24:27], s41 offen offset:16\n\t"
        "buffer_load_dwordx4 v[176:179], v6, s[24:27], s41 offen offset:32\n\t"
        "buffer_load_dwordx2 v[180:181], v6, s[24:27], s41 offen offset:48\n\t"
        "s_mov_b32 s49, m0\n\t"
        "s_mul_i32 s48, s40, 30720\n\t"
        "s_mov_b32 m0, s48\n\t"
        "s_add_u32 s28, s28, s41\n\t"
        "s_addc_u32 s29, s29, 0\n\t"
        "s_sub_u32 s28, s28, s48\n\t"
        "s_subb_u32 s29, s29, 0\n\t"
        "s_add_u32 s30, s48, 28672\n\t"
        "s_mov_b32 s31, s23\n\t"
        "v_lshlrev_b32 v15, 4, v2\n\t"
        "s_mov_b32 s54, 67108864\n\t"
        "s_mov_b32 s55, s23\n\t"
        "s_add_u32 s50, s41, 10752\n\t"
        "s_min_u32 s46, s14, 9\n\t"
        "s_mul_i32 s46, s46, 1792\n\t"
        "s_add_u32 s51, s50, s46\n\t"
        "s_mul_i32 s47, s14, 512\n\t"
        "s_add_u32 s47, s47, s48\n\t"
        "s_bfm_b64 exec, 32, 0\n\t"
        "buffer_load_dwordx4 a[124:127], v15, s[28:31], s47 offen offset:0\n\t"
        "s_mov_b64 exec, -1\n\t"
        "s_mul_i32 s46, s14, 256\n\t"
        "s_add_u32 s46, s46, s41\n\t"
        "s_bfm_b64 exec, 16, 0\n\t"
        "buffer_load_dwordx4 a[120:123], v15, s[24:27], s46 offen offset:3584\n\t"
        "s_mov_b64 exec, -1\n\t"
        "s_add_u32 s4, s24, s41\n\t"
        "s_addc_u32 s5, s25, 0\n\t"
        "s_movk_i32 s6, 3584\n\t"
        "s_mov_b32 s7, s23\n\t"
        "s_add_u32 s4, s4, 3584\n\t"
        "s_addc_u32 s5, s5, 0\n\t"
        "s_mov_b64 s[18:19], s[22:23]\n\t"
        "s_mul_i32 s43, s40, 1792\n\t"
        "s_add_u32 s43, s43, s45\n\t"
        "v_mov_b32 v142, 0\n\t"
        "v_mov_b32 v143, 0\n\t"
        "v_mov_b32 v12, 0x3f80\n\t"
        "v_mov_b32 v13, 0\n\t"
        "v_and_b32 v5, 16, v15\n\t"
        "v_lshlrev_b32 v4, v5, v12\n\t"
        "s_mov_b32 s10, 3\n\t"
        "s_mov_b32 s11, 0x30000\n\t"
        "v_mfma_f32_32x32x8_bf16 a[0:15], v[142:143], v[142:143], 0\n\t"
        "v_cndmask_b32 v128, 0, v4, s[10:11]\n\t"
        "v_mov_b32 v136, v128\n\t"
        "s_lshl_b64 s[46:47], s[10:11], 2\n\t"
        "v_cndmask_b32 v129, 0, v4, s[46:47]\n\t"
        "v_mov_b32 v137, v129\n\t"
        "v_mfma_f32_32x32x8_bf16 a[16:31], v[142:143], v[142:143], 0\n\t"
        "s_lshl_b64 s[46:47], s[10:11], 8\n\t"
        "v_cndmask_b32 v130, 0, v4, s[46:47]\n\t"
        "v_mov_b32 v138, v130\n\t"
        "s_lshl_b64 s[46:47], s[10:11], 10\n\t"
        "v_cndmask_b32 v131, 0, v4, s[46:47]\n\t"
        "v_mfma_f32_32x32x8_bf16 a[32:47], v[142:143], v[142:143], 0\n\t"
        "s_lshl_b64 s[46:47], s[10:11], 4\n\t"
        "v_cndmask_b32 v132, 0, v4, s[46:47]\n\t"
        "v_mov_b32 v139, v132\n\t"
        "s_lshl_b64 s[46:47], s[10:11], 6\n\t"
        "v_cndmask_b32 v133, 0, v4, s[46:47]\n\t"
        "v_mov_b32 v140, v133\n\t"
        "v_mfma_f32_32x32x8_bf16 v[144:159], v[142:143], v[142:143], 0\n\t"
        "s_lshl_b64 s[46:47], s[10:11], 12\n\t"
        "v_cndmask_b32 v134, 0, v4, s[46:47]\n\t"
        "v_mov_b32 v141, v134\n\t"
        "s_lshl_b64 s[46:47], s[10:11], 14\n\t"
        "v_cndmask_b32 v135, 0, v4, s[46:47]\n\t"
        "buffer_load_dwordx4 v15, s[28:31], m0 offen offset:0 lds\n\t"
        "buffer_load_dwordx4 v15, s[28:31], m0 offen offset:1024 lds\n\t"
        "s_lshl2_add_u32 s56, s9, s40\n\t"
        "s_lshl_b32 s56, s56, 15\n\t"
        "s_add_u32 s46, s56, 30720\n\t"
        "v_min_u32 v7, 9, v2\n\t"
        "v_lshl_add_u32 v7, v7, 2, s46\n\t"
        "buffer_load_dword v222, v7, s[52:55], 0 offen sc1\n\t"
        "s_branch .L_skip_%=\n\t"
        ".L_slow_%=:\n\t"
        "s_mov_b32 s58, 12\n\t"
        ".L_repoll_%=:\n\t"
        "buffer_load_dword v223, v7, s[52:55], 0 offen sc1\n\t"
        "s_waitcnt vmcnt(0)\n\t"
        "v_cmp_ne_u32 vcc, v223, v222\n\t"
        "s_nop 1\n\t"
        "s_cbranch_vccz .L_go_%=\n\t"
        "s_sub_u32 s58, s58, 1\n\t"
        "s_cmp_lg_u32 s58, 0\n\t"
        "s_cbranch_scc1 .L_repoll_%=\n\t"
        "s_branch .L_self_%=\n\t"
        ".L_skip_%=:\n\t"
        "v_mov_b32 v9, 0xdddddddd\n\t"
        "v_mov_b32 v58, 0\n\t"
        "v_mov_b32 v10, 0\n\t"
        "v_mov_b32 v103, 0x3f00\n\t"
        "v_mov_b32 v111, v103\n\t"
        "v_mov_b32 v119, v103\n\t"
        "v_mov_b32 v127, v103\n\t"
        "v_mov_b32 v233, v103\n\t"
        "v_mov_b32 v241, v103\n\t"
        "s_mov_b32 s46, 123104\n\t"
        "v_lshl_add_u32 v4, v6, 2, s46\n\t"
        "ds_write_b64 v4, v[12:13] offset:0\n\t"
        "ds_write_b64 v4, v[12:13] offset:14336\n\t"
        "v_bfe_u32 v8, v2, 2, 2\n\t"
        "v_mad_u32_u24 v8, v8, 56, s48\n\t"
        "v_lshl_add_u32 v8, v5, 1, v8\n\t"
        "v_and_b32 v4, 2, v2\n\t"
        "v_lshl_add_u32 v8, v4, 2, v8\n\t"
        "v_and_b32 v4, 16, v2\n\t"
        "v_add_u32 v8, v8, v4\n\t"
        "v_mov_b32 v4, s46\n\t"
        "s_mov_b32 s46, 0x88880000\n\t"
        "s_mov_b32 s47, s46\n\t"
        "v_cndmask_b32 v8, v8, v4, s[46:47]\n\t"
        "s_movk_i32 s46, 448\n\t"
        "v_mad_u32_u24 v8, v3, s46, v8\n\t"
        "s_xor_b32 s46, s40, 1\n\t"
        "s_mul_i32 s46, s46, 30720\n\t"
        "v_add_u32 v213, s46, v15\n\t"
        "s_xor_b32 s46, s40, 2\n\t"
        "s_mul_i32 s46, s46, 30720\n\t"
        "v_add_u32 v214, s46, v15\n\t"
        "s_xor_b32 s46, s40, 3\n\t"
        "s_mul_i32 s46, s46, 30720\n\t"
        "v_add_u32 v215, s46, v15\n\t"
        "v_add_u32 v216, s48, v15\n\t"
        "v_lshl_add_u32 v11, v3, 4, v210\n\t"
        "s_add_u32 s46, s48, 12288\n\t"
        "v_add_u32 v217, s46, v11\n\t"
        "s_lshl_b32 s57, s14, 11\n\t"
        "s_add_u32 s57, s57, s56\n\t"
        "s_cmp_lt_u32 s14, 10\n\t"
        "s_cselect_b32 s46, 1, 0\n\t"
        "s_cselect_b32 s57, s57, 0x7f000000\n\t"
        "s_lshl_b32 s46, s46, s14\n\t"
        "s_mov_b32 s47, 0\n\t"
        "v_add_u32 v252, s57, v15\n\t"
        "v_mov_b32 v4, 0x7f000000\n\t"
        "v_cndmask_b32 v212, v4, v7, s[46:47]\n\t"
        "v_mov_b32 v224, 0\n\t"
        "v_mov_b32 v14, 0x3fb8aa3b\n\t"
        "s_mov_b32 s45, 0x358637bd\n\t"
        "s_mov_b32 s13, 0xffff0000\n\t"
        "s_lshl_b32 s38, s38, 4\n\t"
        "s_waitcnt lgkmcnt(0)\n\t"
        "s_lshr_b32 s44, s44, s38\n\t"
        "s_lshl_b32 s44, s44, 16\n\t"
        "s_and_b32 s46, s44, 0x7fffffff\n\t"
        "s_cmp_gt_u32 s46, 0x41800000\n\t"
        "s_cbranch_scc1 .L_general_%=\n\t"
        "v_mul_f32 v14, s44, v14\n\t"
        "v_mov_b32 v167, 0xc1cc\n\t"
        ".L_start_%=:\n\t"
        "s_waitcnt vmcnt(9)\n\t"
        "v_dot2c_f32_bf16 v58, v48, v48\n\t"
        "v_dot2c_f32_bf16 v58, v49, v49\n\t"
        "v_dot2c_f32_bf16 v58, v50, v50\n\t"
        "v_dot2c_f32_bf16 v58, v51, v51\n\t"
        "v_dot2c_f32_bf16 v58, v52, v52\n\t"
        "v_dot2c_f32_bf16 v58, v53, v53\n\t"
        "v_dot2c_f32_bf16 v58, v54, v54\n\t"
        "v_lshlrev_b32 v32, 16, v48\n\t"
        "v_and_b32 v33, s13, v48\n\t"
        "v_lshlrev_b32 v34, 16, v49\n\t"
        "v_mov_b32 v59, v58\n\t"
        "v_and_b32 v35, s13, v49\n\t"
        "v_lshlrev_b32 v36, 16, v50\n\t"
        "v_permlane32_swap_b32 v58, v59\n\t"
        "v_and_b32 v37, s13, v50\n\t"
        "v_add_f32 v58, v58, v59\n\t"
        "v_lshlrev_b32 v38, 16, v51\n\t"
        "v_sqrt_f32 v58, v58\n\t"
        "v_and_b32 v39, s13, v51\n\t"
        "v_lshlrev_b32 v40, 16, v52\n\t"
        "v_add_f32 v58, s45, v58\n\t"
        "v_and_b32 v41, s13, v52\n\t"
        "v_rcp_f32 v60, v58\n\t"
        "v_lshlrev_b32 v42, 16, v53\n\t"
        "v_and_b32 v43, s13, v53\n\t"
        "v_mul_f32 v60, v60, v14\n\t"
        "v_lshlrev_b32 v44, 16, v54\n\t"
        "v_and_b32 v45, s13, v54\n\t"
        "s_waitcnt vmcnt(7)\n\t"
        "v_pk_mul_f32 v[32:33], v[32:33], v[60:61] op_sel_hi:[1,0]\n\t"
        "v_dot2c_f32_bf16 v10, v168, v168\n\t"
        "v_pk_mul_f32 v[34:35], v[34:35], v[60:61] op_sel_hi:[1,0]\n\t"
        "v_dot2c_f32_bf16 v10, v169, v169\n\t"
        "v_pk_mul_f32 v[36:37], v[36:37], v[60:61] op_sel_hi:[1,0]\n\t"
        "v_dot2c_f32_bf16 v10, v170, v170\n\t"
        "v_pk_mul_f32 v[38:39], v[38:39], v[60:61] op_sel_hi:[1,0]\n\t"
        "v_dot2c_f32_bf16 v10, v171, v171\n\t"
        "v_pk_mul_f32 v[40:41], v[40:41], v[60:61] op_sel_hi:[1,0]\n\t"
        "v_dot2c_f32_bf16 v10, v172, v172\n\t"
        "v_pk_mul_f32 v[42:43], v[42:43], v[60:61] op_sel_hi:[1,0]\n\t"
        "v_dot2c_f32_bf16 v10, v173, v173\n\t"
        "v_pk_mul_f32 v[44:45], v[44:45], v[60:61] op_sel_hi:[1,0]\n\t"
        "v_dot2c_f32_bf16 v10, v174, v174\n\t"
        "v_dot2c_f32_bf16 v10, v175, v175\n\t"
        "s_waitcnt vmcnt(5)\n\t"
        "v_dot2c_f32_bf16 v10, v176, v176\n\t"
        "v_dot2c_f32_bf16 v10, v177, v177\n\t"
        "v_dot2c_f32_bf16 v10, v178, v178\n\t"
        "v_dot2c_f32_bf16 v10, v179, v179\n\t"
        "v_dot2c_f32_bf16 v10, v180, v180\n\t"
        "v_dot2c_f32_bf16 v10, v181, v181\n\t"
        "v_mfma_f32_32x32x16_bf16 v[64:79], v[128:131], v[168:171], 0\n\t"
        "buffer_load_dwordx4 v15, s[28:31], m0 offen offset:2048 lds\n\t"
        "buffer_load_dwordx4 v15, s[28:31], m0 offen offset:3072 lds\n\t"
        "s_addk_i32 m0, 4096\n\t"
        "v_cvt_pk_bf16_f32 v160, v32, v33\n\t"
        "v_cvt_pk_bf16_f32 v161, v34, v35\n\t"
        "v_cvt_pk_bf16_f32 v162, v36, v37\n\t"
        "v_cvt_pk_bf16_f32 v163, v38, v39\n\t"
        "v_sqrt_f32 v10, v10\n\t"
        "v_mfma_f32_32x32x16_bf16 v[80:95], v[136:139], v[176:179], 0\n\t"
        "ds_write_b128 v216, v[160:163] offset:28672\n\t"
        "v_add_f32 v10, s45, v10\n\t"
        "v_cvt_pk_bf16_f32 v164, v40, v41\n\t"
        "v_cvt_pk_bf16_f32 v165, v42, v43\n\t"
        "v_mfma_f32_32x32x16_bf16 v[64:79], v[132:135], v[172:175], v[64:79]\n\t"
        "buffer_load_dwordx4 v[182:185], v6, s[4:7], 0 offen offset:0\n\t"
        "buffer_load_dwordx4 v[186:189], v6, s[4:7], 0 offen offset:16\n\t"
        "buffer_load_dwordx4 v[190:193], v6, s[4:7], 0 offen offset:32\n\t"
        "buffer_load_dwordx2 v[194:195], v6, s[4:7], 0 offen offset:48\n\t"
        "v_rcp_f32 v12, v10\n\t"
        "v_cvt_pk_bf16_f32 v166, v44, v45\n\t"
        "v_mfma_f32_32x32x8_bf16 v[80:95], v[140:141], v[180:181], v[80:95]\n\t"
        "ds_write_b128 v216, v[164:167] offset:29696\n\t"
        "v_mov_b32 v13, v12\n\t"
        "s_nop 1\n\t"
        "v_permlane32_swap_b32 v12, v13\n\t"
        "v_pk_mul_f32 v[64:65], v[64:65], v[12:13] op_sel_hi:[1,0]\n\t"
        "v_pk_mul_f32 v[66:67], v[66:67], v[12:13] op_sel_hi:[1,0]\n\t"
        "v_pk_mul_f32 v[68:69], v[68:69], v[12:13] op_sel_hi:[1,0]\n\t"
        "v_pk_mul_f32 v[70:71], v[70:71], v[12:13] op_sel_hi:[1,0]\n\t"
        "v_pk_mul_f32 v[72:73], v[72:73], v[12:13] op_sel:[0,1] op_sel_hi:[1,1]\n\t"
        "v_pk_mul_f32 v[74:75], v[74:75], v[12:13] op_sel:[0,1] op_sel_hi:[1,1]\n\t"
        "v_pk_mul_f32 v[76:77], v[76:77], v[12:13] op_sel:[0,1] op_sel_hi:[1,1]\n\t"
        "v_pk_mul_f32 v[78:79], v[78:79], v[12:13] op_sel:[0,1] op_sel_hi:[1,1]\n\t"
        "v_cvt_pk_bf16_f32 v96, v64, v65\n\t"
        "v_cvt_pk_bf16_f32 v97, v66, v67\n\t"
        "v_cvt_pk_bf16_f32 v98, v68, v69\n\t"
        "v_cvt_pk_bf16_f32 v99, v70, v71\n\t"
        "v_pk_mul_f32 v[80:81], v[80:81], v[12:13] op_sel_hi:[1,0]\n\t"
        "v_pk_mul_f32 v[82:83], v[82:83], v[12:13] op_sel_hi:[1,0]\n\t"
        "v_pk_mul_f32 v[84:85], v[84:85], v[12:13] op_sel_hi:[1,0]\n\t"
        "v_pk_mul_f32 v[88:89], v[88:89], v[12:13] op_sel:[0,1] op_sel_hi:[1,1]\n\t"
        "v_pk_mul_f32 v[90:91], v[90:91], v[12:13] op_sel:[0,1] op_sel_hi:[1,1]\n\t"
        "v_pk_mul_f32 v[92:93], v[92:93], v[12:13] op_sel:[0,1] op_sel_hi:[1,1]\n\t"
        "v_mfma_f32_32x32x16_bf16 v[16:31], v[96:99], v[160:163], 0\n\t"
        "v_cvt_pk_bf16_f32 v100, v80, v81\n\t"
        "v_cvt_pk_bf16_f32 v101, v82, v83\n\t"
        "v_cvt_pk_bf16_f32 v102, v84, v85\n\t"
        "v_cvt_pk_bf16_f32 v104, v72, v73\n\t"
        "v_cvt_pk_bf16_f32 v105, v74, v75\n\t"
        "v_cvt_pk_bf16_f32 v106, v76, v77\n\t"
        "v_mfma_f32_32x32x16_bf16 v[16:31], v[100:103], v[164:167], v[16:31]\n\t"
        "buffer_load_dwordx4 v15, s[28:31], m0 offen offset:0 lds\n\t"
        "buffer_load_dwordx4 v15, s[28:31], m0 offen offset:1024 lds\n\t"
        "buffer_load_dwordx4 v15, s[28:31], m0 offen offset:2048 lds\n\t"
        "buffer_load_dwordx4 v15, s[28:31], m0 offen offset:3072 lds\n\t"
        "v_cvt_pk_bf16_f32 v107, v78, v79\n\t"
        "v_cvt_pk_bf16_f32 v108, v88, v89\n\t"
        "v_cvt_pk_bf16_f32 v109, v90, v91\n\t"
        "v_cvt_pk_bf16_f32 v110, v92, v93\n\t"
        "v_mfma_f32_32x32x16_bf16 v[32:47], v[104:107], v[160:163], 0\n\t"
        "s_waitcnt vmcnt(11) lgkmcnt(0)\n\t"
        "ds_read_b64_tr_b16 a[72:73], v8 offset:0\n\t"
        "ds_read_b64_tr_b16 a[74:75], v8 offset:224\n\t"
        "ds_read_b64_tr_b16 a[76:77], v8 offset:896\n\t"
        "ds_read_b64_tr_b16 a[78:79], v8 offset:1120\n\t"
        "v_exp_f32 v16, v16\n\t"
        "v_exp_f32 v17, v17\n\t"
        "v_exp_f32 v18, v18\n\t"
        "v_exp_f32 v19, v19\n\t"
        "v_exp_f32 v20, v20\n\t"
        "v_mfma_f32_32x32x16_bf16 v[32:47], v[108:111], v[164:167], v[32:47]\n\t"
        "s_barrier\n\t"
        "ds_read_b128 a[48:51], v215 offset:28672\n\t"
        "ds_read_b128 a[52:55], v215 offset:29696\n\t"
        "buffer_load_dwordx4 v[242:245], v210, s[24:27], s51 offen\n\t"
        "buffer_load_dwordx3 v[246:248], v211, s[24:27], s51 offen offset:32\n\t"
        "v_exp_f32 v21, v21\n\t"
        "v_exp_f32 v22, v22\n\t"
        "v_exp_f32 v23, v23\n\t"
        "v_exp_f32 v24, v24\n\t"
        "v_exp_f32 v25, v25\n\t"
        "v_exp_f32 v26, v26\n\t"
        "s_waitcnt lgkmcnt(2)\n\t"
        "v_smfmac_f32_32x32x32_bf16 a[0:15], a[72:75], v[16:23], v9\n\t"
        "s_waitcnt vmcnt(10)\n\t"
        "ds_read_b64_tr_b16 a[80:81], v8 offset:1792\n\t"
        "ds_read_b64_tr_b16 a[82:83], v8 offset:2016\n\t"
        "ds_read_b64_tr_b16 a[84:85], v8 offset:2688\n\t"
        "ds_read_b64_tr_b16 a[86:87], v8 offset:2912\n\t"
        "v_exp_f32 v27, v27\n\t"
        "v_exp_f32 v28, v28\n\t"
        "s_add_u32 s4, s4, 3584\n\t"
        "s_addc_u32 s5, s5, 0\n\t"
        "v_exp_f32 v29, v29\n\t"
        "v_exp_f32 v30, v30\n\t"
        "v_exp_f32 v31, v31\n\t"
        "s_waitcnt lgkmcnt(4)\n\t"
        "v_mfma_f32_32x32x16_bf16 v[48:63], v[100:103], a[52:55], 0\n\t"
        "ds_read_b128 a[56:59], v214 offset:28672\n\t"
        "ds_read_b128 a[60:63], v214 offset:29696\n\t"
        "v_exp_f32 v32, v32\n\t"
        "v_exp_f32 v33, v33\n\t"
        "v_exp_f32 v34, v34\n\t"
        "v_exp_f32 v35, v35\n\t"
        "v_mfma_f32_32x32x16_bf16 v[48:63], v[96:99], a[48:51], v[48:63]\n\t"
        "ds_read_b128 a[64:67], v213 offset:28672\n\t"
        "ds_read_b128 a[68:71], v213 offset:29696\n\t"
        "v_exp_f32 v36, v36\n\t"
        "v_exp_f32 v37, v37\n\t"
        "v_exp_f32 v38, v38\n\t"
        "v_exp_f32 v39, v39\n\t"
        "v_smfmac_f32_32x32x32_bf16 a[0:15], a[76:79], v[24:31], v9\n\t"
        "s_waitcnt lgkmcnt(4)\n\t"
        "buffer_load_dwordx4 v[196:199], v6, s[4:7], 0 offen offset:0\n\t"
        "buffer_load_dwordx4 v[200:203], v6, s[4:7], 0 offen offset:16\n\t"
        "buffer_load_dwordx4 v[204:207], v6, s[4:7], 0 offen offset:32\n\t"
        "buffer_load_dwordx2 v[208:209], v6, s[4:7], 0 offen offset:48\n\t"
        "v_exp_f32 v40, v40\n\t"
        "v_exp_f32 v41, v41\n\t"
        "v_exp_f32 v42, v42\n\t"
        "v_exp_f32 v43, v43\n\t"
        "v_smfmac_f32_32x32x32_bf16 a[0:15], a[80:83], v[32:39], v9\n\t"
        "s_waitcnt vmcnt(10)\n\t"
        "v_exp_f32 v44, v44\n\t"
        "v_exp_f32 v45, v45\n\t"
        "v_exp_f32 v46, v46\n\t"
        "v_exp_f32 v47, v47\n\t"
        "v_dot2_f32_bf16 v10, v182, v182, 0\n\t"
        "v_mfma_f32_32x32x16_bf16 v[16:31], v[108:111], a[52:55], 0\n\t"
        "v_exp_f32 v48, v48\n\t"
        "v_exp_f32 v49, v49\n\t"
        "v_exp_f32 v50, v50\n\t"
        "v_exp_f32 v51, v51\n\t"
        "v_mfma_f32_32x32x16_bf16 v[16:31], v[104:107], a[48:51], v[16:31]\n\t"
        "s_addk_i32 m0, 4096\n\t"
        "v_exp_f32 v52, v52\n\t"
        "v_exp_f32 v53, v53\n\t"
        "v_exp_f32 v54, v54\n\t"
        "v_exp_f32 v55, v55\n\t"
        "v_smfmac_f32_32x32x32_bf16 a[0:15], a[84:87], v[40:47], v9\n\t"
        "buffer_load_dwordx4 v15, s[28:31], m0 offen offset:0 lds\n\t"
        "buffer_load_dwordx4 v15, s[28:31], m0 offen offset:1024 lds\n\t"
        "buffer_load_dwordx4 v15, s[28:31], m0 offen offset:2048 lds\n\t"
        "buffer_load_dwordx4 v15, s[28:31], m0 offen offset:3072 lds\n\t"
        "v_exp_f32 v56, v56\n\t"
        "v_exp_f32 v57, v57\n\t"
        "v_exp_f32 v58, v58\n\t"
        "v_exp_f32 v59, v59\n\t"
        "v_dot2c_f32_bf16 v10, v183, v183\n\t"
        "v_dot2c_f32_bf16 v10, v184, v184\n\t"
        "v_dot2c_f32_bf16 v10, v185, v185\n\t"
        "v_dot2c_f32_bf16 v10, v186, v186\n\t"
        "v_dot2c_f32_bf16 v10, v187, v187\n\t"
        "v_dot2c_f32_bf16 v10, v188, v188\n\t"
        "v_dot2c_f32_bf16 v10, v189, v189\n\t"
        "v_smfmac_f32_32x32x32_bf16 a[16:31], a[72:75], v[48:55], v9\n\t"
        "s_waitcnt lgkmcnt(0)\n\t"
        "v_exp_f32 v60, v60\n\t"
        "v_exp_f32 v61, v61\n\t"
        "v_exp_f32 v62, v62\n\t"
        "v_exp_f32 v63, v63\n\t"
        "v_dot2c_f32_bf16 v10, v190, v190\n\t"
        "v_dot2c_f32_bf16 v10, v191, v191\n\t"
        "v_dot2c_f32_bf16 v10, v192, v192\n\t"
        "v_dot2c_f32_bf16 v10, v193, v193\n\t"
        "v_dot2c_f32_bf16 v10, v194, v194\n\t"
        "v_dot2c_f32_bf16 v10, v195, v195\n\t"
        "v_mfma_f32_32x32x16_bf16 v[32:47], v[100:103], a[60:63], 0\n\t"
        "v_exp_f32 v16, v16\n\t"
        "v_exp_f32 v17, v17\n\t"
        "v_exp_f32 v18, v18\n\t"
        "v_mfma_f32_32x32x16_bf16 v[32:47], v[96:99], a[56:59], v[32:47]\n\t"
        "v_exp_f32 v19, v19\n\t"
        "v_exp_f32 v20, v20\n\t"
        "v_sqrt_f32 v10, v10\n\t"
        "v_exp_f32 v21, v21\n\t"
        "v_mfma_f32_32x32x16_bf16 v[64:79], v[128:131], v[182:185], 0\n\t"
        "v_exp_f32 v22, v22\n\t"
        "v_exp_f32 v23, v23\n\t"
        "v_add_f32 v10, s45, v10\n\t"
        "v_exp_f32 v24, v24\n\t"
        "v_mfma_f32_32x32x16_bf16 v[80:95], v[136:139], v[190:193], 0\n\t"
        "v_exp_f32 v25, v25\n\t"
        "v_exp_f32 v26, v26\n\t"
        "v_rcp_f32 v12, v10\n\t"
        "v_exp_f32 v27, v27\n\t"
        "v_smfmac_f32_32x32x32_bf16 a[16:31], a[76:79], v[56:63], v9\n\t"
        "v_exp_f32 v28, v28\n\t"
        "v_exp_f32 v29, v29\n\t"
        "v_mov_b32 v13, v12\n\t"
        "v_smfmac_f32_32x32x32_bf16 a[16:31], a[80:83], v[16:23], v9\n\t"
        "v_exp_f32 v30, v30\n\t"
        "v_exp_f32 v31, v31\n\t"
        "v_permlane32_swap_b32 v12, v13\n\t"
        "v_mfma_f32_32x32x16_bf16 v[48:63], v[108:111], a[60:63], 0\n\t"
        "s_waitcnt vmcnt(11)\n\t"
        "v_exp_f32 v32, v32\n\t"
        "v_exp_f32 v33, v33\n\t"
        "v_exp_f32 v34, v34\n\t"
        "v_mfma_f32_32x32x16_bf16 v[48:63], v[104:107], a[56:59], v[48:63]\n\t"
        "v_exp_f32 v35, v35\n\t"
        "v_exp_f32 v36, v36\n\t"
        "v_exp_f32 v37, v37\n\t"
        "v_mfma_f32_32x32x16_bf16 v[64:79], v[132:135], v[186:189], v[64:79]\n\t"
        "v_exp_f32 v38, v38\n\t"
        "v_exp_f32 v39, v39\n\t"
        "v_exp_f32 v40, v40\n\t"
        "v_mfma_f32_32x32x8_bf16 v[80:95], v[140:141], v[194:195], v[80:95]\n\t"
        "ds_read_b64_tr_b16 a[88:89], v8 offset:3584\n\t"
        "v_exp_f32 v41, v41\n\t"
        "v_exp_f32 v42, v42\n\t"
        "v_exp_f32 v43, v43\n\t"
        "v_smfmac_f32_32x32x32_bf16 a[16:31], a[84:87], v[24:31], v9\n\t"
        "ds_read_b64_tr_b16 a[90:91], v8 offset:3808\n\t"
        "v_exp_f32 v44, v44\n\t"
        "v_exp_f32 v45, v45\n\t"
        "v_smfmac_f32_32x32x32_bf16 a[32:47], a[72:75], v[32:39], v9\n\t"
        "ds_read_b64_tr_b16 a[92:93], v8 offset:4480\n\t"
        "v_exp_f32 v46, v46\n\t"
        "v_exp_f32 v47, v47\n\t"
        "v_mfma_f32_32x32x16_bf16 v[16:31], v[100:103], a[68:71], 0\n\t"
        "ds_read_b64_tr_b16 a[94:95], v8 offset:4704\n\t"
        "v_exp_f32 v48, v48\n\t"
        "v_exp_f32 v49, v49\n\t"
        "v_exp_f32 v50, v50\n\t"
        "v_exp_f32 v51, v51\n\t"
        "v_pk_mul_f32 v[64:65], v[64:65], v[12:13] op_sel_hi:[1,0]\n\t"
        "v_pk_mul_f32 v[66:67], v[66:67], v[12:13] op_sel_hi:[1,0]\n\t"
        "v_pk_mul_f32 v[68:69], v[68:69], v[12:13] op_sel_hi:[1,0]\n\t"
        "v_pk_mul_f32 v[70:71], v[70:71], v[12:13] op_sel_hi:[1,0]\n\t"
        "v_mfma_f32_32x32x16_bf16 v[16:31], v[96:99], a[64:67], v[16:31]\n\t"
        "ds_read_b64_tr_b16 a[96:97], v8 offset:5376\n\t"
        "v_exp_f32 v52, v52\n\t"
        "v_exp_f32 v53, v53\n\t"
        "v_exp_f32 v54, v54\n\t"
        "v_exp_f32 v55, v55\n\t"
        "v_pk_mul_f32 v[80:81], v[80:81], v[12:13] op_sel_hi:[1,0]\n\t"
        "v_pk_mul_f32 v[82:83], v[82:83], v[12:13] op_sel_hi:[1,0]\n\t"
        "v_pk_mul_f32 v[84:85], v[84:85], v[12:13] op_sel_hi:[1,0]\n\t"
        "v_smfmac_f32_32x32x32_bf16 a[32:47], a[76:79], v[40:47], v9\n\t"
        "s_waitcnt vmcnt(8)\n\t"
        "v_exp_f32 v56, v56\n\t"
        "v_exp_f32 v57, v57\n\t"
        "v_exp_f32 v58, v58\n\t"
        "v_exp_f32 v59, v59\n\t"
        "v_dot2c_f32_bf16 v224, v242, v242\n\t"
        "v_dot2c_f32_bf16 v224, v243, v243\n\t"
        "v_dot2c_f32_bf16 v224, v244, v244\n\t"
        "v_dot2c_f32_bf16 v224, v245, v245\n\t"
        "v_dot2c_f32_bf16 v224, v246, v246\n\t"
        "v_dot2c_f32_bf16 v224, v247, v247\n\t"
        "v_dot2c_f32_bf16 v224, v248, v248\n\t"
        "v_pk_mul_f32 v[72:73], v[72:73], v[12:13] op_sel:[0,1] op_sel_hi:[1,1]\n\t"
        "v_pk_mul_f32 v[74:75], v[74:75], v[12:13] op_sel:[0,1] op_sel_hi:[1,1]\n\t"
        "v_pk_mul_f32 v[76:77], v[76:77], v[12:13] op_sel:[0,1] op_sel_hi:[1,1]\n\t"
        "v_pk_mul_f32 v[78:79], v[78:79], v[12:13] op_sel:[0,1] op_sel_hi:[1,1]\n\t"
        "v_smfmac_f32_32x32x32_bf16 a[32:47], a[80:83], v[48:55], v9\n\t"
        "v_exp_f32 v60, v60\n\t"
        "v_cvt_pk_bf16_f32 v112, v64, v65\n\t"
        "v_exp_f32 v61, v61\n\t"
        "v_cvt_pk_bf16_f32 v113, v66, v67\n\t"
        "v_exp_f32 v62, v62\n\t"
        "v_lshlrev_b32 v182, 16, v242\n\t"
        "v_exp_f32 v63, v63\n\t"
        "v_and_b32 v183, s13, v242\n\t"
        "v_mov_b32 v225, v224\n\t"
        "v_lshlrev_b32 v184, 16, v243\n\t"
        "v_and_b32 v185, s13, v243\n\t"
        "v_permlane32_swap_b32 v224, v225\n\t"
        "v_pk_mul_f32 v[88:89], v[88:89], v[12:13] op_sel:[0,1] op_sel_hi:[1,1]\n\t"
        "v_pk_mul_f32 v[90:91], v[90:91], v[12:13] op_sel:[0,1] op_sel_hi:[1,1]\n\t"
        "v_pk_mul_f32 v[92:93], v[92:93], v[12:13] op_sel:[0,1] op_sel_hi:[1,1]\n\t"
        "v_mfma_f32_32x32x16_bf16 v[32:47], v[108:111], a[68:71], 0\n\t"
        "v_exp_f32 v16, v16\n\t"
        "v_cvt_pk_bf16_f32 v114, v68, v69\n\t"
        "v_exp_f32 v17, v17\n\t"
        "v_cvt_pk_bf16_f32 v115, v70, v71\n\t"
        "v_exp_f32 v18, v18\n\t"
        "v_cvt_pk_bf16_f32 v116, v80, v81\n\t"
        "v_exp_f32 v19, v19\n\t"
        "v_add_f32 v224, v224, v225\n\t"
        "v_sqrt_f32 v224, v224\n\t"
        "v_lshlrev_b32 v186, 16, v244\n\t"
        "v_and_b32 v187, s13, v244\n\t"
        "v_add_f32 v224, s45, v224\n\t"
        "v_mfma_f32_32x32x16_bf16 v[32:47], v[104:107], a[64:67], v[32:47]\n\t"
        "v_exp_f32 v20, v20\n\t"
        "v_cvt_pk_bf16_f32 v117, v82, v83\n\t"
        "v_exp_f32 v21, v21\n\t"
        "v_cvt_pk_bf16_f32 v118, v84, v85\n\t"
        "v_exp_f32 v22, v22\n\t"
        "v_cvt_pk_bf16_f32 v120, v72, v73\n\t"
        "v_exp_f32 v23, v23\n\t"
        "v_rcp_f32 v250, v224\n\t"
        "v_lshlrev_b32 v188, 16, v245\n\t"
        "v_and_b32 v189, s13, v245\n\t"
        "v_lshlrev_b32 v190, 16, v246\n\t"
        "v_and_b32 v191, s13, v246\n\t"
        "v_pk_mul_f32 v[182:183], v[182:183], v[250:251] op_sel_hi:[1,0]\n\t"
        "v_pk_mul_f32 v[184:185], v[184:185], v[250:251] op_sel_hi:[1,0]\n\t"
        "v_pk_mul_f32 v[186:187], v[186:187], v[250:251] op_sel_hi:[1,0]\n\t"
        "v_pk_mul_f32 v[188:189], v[188:189], v[250:251] op_sel_hi:[1,0]\n\t"
        "v_smfmac_f32_32x32x32_bf16 a[32:47], a[84:87], v[56:63], v9\n\t"
        "v_exp_f32 v24, v24\n\t"
        "v_cvt_pk_bf16_f32 v121, v74, v75\n\t"
        "v_exp_f32 v25, v25\n\t"
        "v_cvt_pk_bf16_f32 v122, v76, v77\n\t"
        "v_exp_f32 v26, v26\n\t"
        "v_cvt_pk_bf16_f32 v123, v78, v79\n\t"
        "v_exp_f32 v27, v27\n\t"
        "v_lshlrev_b32 v192, 16, v247\n\t"
        "v_and_b32 v193, s13, v247\n\t"
        "v_lshlrev_b32 v194, 16, v248\n\t"
        "v_and_b32 v195, s13, v248\n\t"
        "v_pk_mul_f32 v[190:191], v[190:191], v[250:251] op_sel_hi:[1,0]\n\t"
        "v_pk_mul_f32 v[192:193], v[192:193], v[250:251] op_sel_hi:[1,0]\n\t"
        "v_pk_mul_f32 v[194:195], v[194:195], v[250:251] op_sel_hi:[1,0]\n\t"
        "v_smfmac_f32_32x32x32_bf16 v[144:159], a[72:75], v[16:23], v9\n\t"
        "v_exp_f32 v28, v28\n\t"
        "v_cvt_pk_bf16_f32 v124, v88, v89\n\t"
        "v_exp_f32 v29, v29\n\t"
        "v_cvt_pk_bf16_f32 v125, v90, v91\n\t"
        "v_exp_f32 v30, v30\n\t"
        "v_cvt_pk_bf16_f32 v126, v92, v93\n\t"
        "v_exp_f32 v31, v31\n\t"
        "v_cvt_pk_bf16_f32 v226, v182, v183\n\t"
        "v_cvt_pk_bf16_f32 v227, v184, v185\n\t"
        "v_cvt_pk_bf16_f32 v228, v186, v187\n\t"
        "v_cvt_pk_bf16_f32 v229, v188, v189\n\t"
        "v_mfma_f32_32x32x16_bf16 v[48:63], v[116:119], v[164:167], 0\n\t"
        "ds_read_b64_tr_b16 a[98:99], v8 offset:5600\n\t"
        "v_exp_f32 v32, v32\n\t"
        "v_cvt_pk_bf16_f32 v230, v190, v191\n\t"
        "v_exp_f32 v33, v33\n\t"
        "v_cvt_pk_bf16_f32 v231, v192, v193\n\t"
        "v_exp_f32 v34, v34\n\t"
        "v_cvt_pk_bf16_f32 v232, v194, v195\n\t"
        "v_exp_f32 v35, v35\n\t"
        "v_add_u32 v222, 1, v222\n\t"
        "v_mfma_f32_32x32x16_bf16 v[48:63], v[112:115], v[160:163], v[48:63]\n\t"
        "ds_read_b64_tr_b16 a[100:101], v8 offset:6272\n\t"
        "buffer_store_dwordx4 v[226:229], v252, s[52:55], 0 offen offset:0 sc1\n\t"
        "buffer_store_dwordx4 v[230:233], v252, s[52:55], 0 offen offset:1024 sc1\n\t"
        "v_exp_f32 v36, v36\n\t"
        "v_exp_f32 v37, v37\n\t"
        "v_exp_f32 v38, v38\n\t"
        "v_exp_f32 v39, v39\n\t"
        "v_smfmac_f32_32x32x32_bf16 v[144:159], a[76:79], v[24:31], v9\n\t"
        "ds_read_b64_tr_b16 a[102:103], v8 offset:6496\n\t"
        "v_exp_f32 v40, v40\n\t"
        "v_exp_f32 v41, v41\n\t"
        "v_exp_f32 v42, v42\n\t"
        "v_exp_f32 v43, v43\n\t"
        "v_smfmac_f32_32x32x32_bf16 v[144:159], a[80:83], v[32:39], v9\n\t"
        "v_exp_f32 v44, v44\n\t"
        "v_exp_f32 v45, v45\n\t"
        "v_exp_f32 v46, v46\n\t"
        "v_exp_f32 v47, v47\n\t"
        "v_mfma_f32_32x32x16_bf16 v[16:31], v[124:127], v[164:167], 0\n\t"
        "v_exp_f32 v48, v48\n\t"
        "v_exp_f32 v49, v49\n\t"
        "v_exp_f32 v50, v50\n\t"
        "v_exp_f32 v51, v51\n\t"
        "v_mfma_f32_32x32x16_bf16 v[16:31], v[120:123], v[160:163], v[16:31]\n\t"
        "s_waitcnt lgkmcnt(0)\n\t"
        "v_exp_f32 v52, v52\n\t"
        "v_exp_f32 v53, v53\n\t"
        "v_exp_f32 v54, v54\n\t"
        "v_exp_f32 v55, v55\n\t"
        "v_smfmac_f32_32x32x32_bf16 v[144:159], a[84:87], v[40:47], v9\n\t"
        "v_exp_f32 v56, v56\n\t"
        "v_exp_f32 v57, v57\n\t"
        "v_exp_f32 v58, v58\n\t"
        "v_exp_f32 v59, v59\n\t"
        "v_smfmac_f32_32x32x32_bf16 a[0:15], a[88:91], v[48:55], v9\n\t"
        "v_exp_f32 v60, v60\n\t"
        "v_exp_f32 v61, v61\n\t"
        "v_exp_f32 v62, v62\n\t"
        "v_exp_f32 v63, v63\n\t"
        "v_mfma_f32_32x32x16_bf16 v[32:47], v[116:119], a[52:55], 0\n\t"
        "v_exp_f32 v16, v16\n\t"
        "v_exp_f32 v17, v17\n\t"
        "v_exp_f32 v18, v18\n\t"
        "v_exp_f32 v19, v19\n\t"
        "v_mfma_f32_32x32x16_bf16 v[32:47], v[112:115], a[48:51], v[32:47]\n\t"
        "v_exp_f32 v20, v20\n\t"
        "v_exp_f32 v21, v21\n\t"
        "v_exp_f32 v22, v22\n\t"
        "v_exp_f32 v23, v23\n\t"
        "v_smfmac_f32_32x32x32_bf16 a[0:15], a[92:95], v[56:63], v9\n\t"
        "v_exp_f32 v24, v24\n\t"
        "v_exp_f32 v25, v25\n\t"
        "v_exp_f32 v26, v26\n\t"
        "v_exp_f32 v27, v27\n\t"
        "v_smfmac_f32_32x32x32_bf16 a[0:15], a[96:99], v[16:23], v9\n\t"
        "s_waitcnt vmcnt(4)\n\t"
        "v_exp_f32 v28, v28\n\t"
        "v_exp_f32 v29, v29\n\t"
        "v_exp_f32 v30, v30\n\t"
        "v_exp_f32 v31, v31\n\t"
        "v_dot2_f32_bf16 v10, v196, v196, 0\n\t"
        "v_mfma_f32_32x32x16_bf16 v[48:63], v[124:127], a[52:55], 0\n\t"
        "v_exp_f32 v32, v32\n\t"
        "v_exp_f32 v33, v33\n\t"
        "v_exp_f32 v34, v34\n\t"
        "v_exp_f32 v35, v35\n\t"
        "v_mfma_f32_32x32x16_bf16 v[48:63], v[120:123], a[48:51], v[48:63]\n\t"
        "v_exp_f32 v36, v36\n\t"
        "v_exp_f32 v37, v37\n\t"
        "v_exp_f32 v38, v38\n\t"
        "v_exp_f32 v39, v39\n\t"
        "v_smfmac_f32_32x32x32_bf16 a[0:15], a[100:103], v[24:31], v9\n\t"
        "v_exp_f32 v40, v40\n\t"
        "v_exp_f32 v41, v41\n\t"
        "v_exp_f32 v42, v42\n\t"
        "v_exp_f32 v43, v43\n\t"
        "v_dot2c_f32_bf16 v10, v197, v197\n\t"
        "v_dot2c_f32_bf16 v10, v198, v198\n\t"
        "v_dot2c_f32_bf16 v10, v199, v199\n\t"
        "v_dot2c_f32_bf16 v10, v200, v200\n\t"
        "v_dot2c_f32_bf16 v10, v201, v201\n\t"
        "v_dot2c_f32_bf16 v10, v202, v202\n\t"
        "v_dot2c_f32_bf16 v10, v203, v203\n\t"
        "v_smfmac_f32_32x32x32_bf16 a[16:31], a[88:91], v[32:39], v9\n\t"
        "s_waitcnt vmcnt(0)\n\t"
        "buffer_store_dword v222, v212, s[52:55], 0 offen sc1\n\t"
        "v_exp_f32 v44, v44\n\t"
        "v_exp_f32 v45, v45\n\t"
        "v_exp_f32 v46, v46\n\t"
        "v_exp_f32 v47, v47\n\t"
        "v_dot2c_f32_bf16 v10, v204, v204\n\t"
        "v_dot2c_f32_bf16 v10, v205, v205\n\t"
        "v_dot2c_f32_bf16 v10, v206, v206\n\t"
        "v_dot2c_f32_bf16 v10, v207, v207\n\t"
        "v_dot2c_f32_bf16 v10, v208, v208\n\t"
        "v_dot2c_f32_bf16 v10, v209, v209\n\t"
        "v_mfma_f32_32x32x16_bf16 v[16:31], v[116:119], a[60:63], 0\n\t"
        "v_exp_f32 v48, v48\n\t"
        "v_exp_f32 v49, v49\n\t"
        "v_exp_f32 v50, v50\n\t"
        "v_mfma_f32_32x32x16_bf16 v[16:31], v[112:115], a[56:59], v[16:31]\n\t"
        "s_addk_i32 m0, 4096\n\t"
        "v_exp_f32 v51, v51\n\t"
        "v_exp_f32 v52, v52\n\t"
        "v_sqrt_f32 v10, v10\n\t"
        "v_exp_f32 v53, v53\n\t"
        "v_mfma_f32_32x32x16_bf16 v[64:79], v[128:131], v[196:199], 0\n\t"
        "v_exp_f32 v54, v54\n\t"
        "v_exp_f32 v55, v55\n\t"
        "v_add_f32 v10, s45, v10\n\t"
        "v_exp_f32 v56, v56\n\t"
        "v_mfma_f32_32x32x16_bf16 v[80:95], v[136:139], v[204:207], 0\n\t"
        "v_exp_f32 v57, v57\n\t"
        "v_exp_f32 v58, v58\n\t"
        "v_rcp_f32 v12, v10\n\t"
        "v_exp_f32 v59, v59\n\t"
        "v_smfmac_f32_32x32x32_bf16 a[16:31], a[92:95], v[40:47], v9\n\t"
        "buffer_load_dwordx4 v15, s[28:31], m0 offen offset:0 lds\n\t"
        "buffer_load_dwordx4 v15, s[28:31], m0 offen offset:1024 lds\n\t"
        "buffer_load_dwordx4 v15, s[28:31], m0 offen offset:2048 lds\n\t"
        "buffer_load_dwordx4 v15, s[28:31], m0 offen offset:3072 lds\n\t"
        "v_exp_f32 v60, v60\n\t"
        "v_exp_f32 v61, v61\n\t"
        "v_mov_b32 v13, v12\n\t"
        "v_smfmac_f32_32x32x32_bf16 a[16:31], a[96:99], v[48:55], v9\n\t"
        "v_exp_f32 v62, v62\n\t"
        "v_exp_f32 v63, v63\n\t"
        "v_permlane32_swap_b32 v12, v13\n\t"
        "v_mfma_f32_32x32x16_bf16 v[32:47], v[124:127], a[60:63], 0\n\t"
        "s_waitcnt vmcnt(5)\n\t"
        "v_exp_f32 v16, v16\n\t"
        "v_exp_f32 v17, v17\n\t"
        "v_exp_f32 v18, v18\n\t"
        "v_mfma_f32_32x32x16_bf16 v[32:47], v[120:123], a[56:59], v[32:47]\n\t"
        "v_exp_f32 v19, v19\n\t"
        "v_exp_f32 v20, v20\n\t"
        "v_exp_f32 v21, v21\n\t"
        "v_mfma_f32_32x32x16_bf16 v[64:79], v[132:135], v[200:203], v[64:79]\n\t"
        "v_exp_f32 v22, v22\n\t"
        "v_exp_f32 v23, v23\n\t"
        "v_exp_f32 v24, v24\n\t"
        "v_mfma_f32_32x32x8_bf16 v[80:95], v[140:141], v[208:209], v[80:95]\n\t"
        "ds_read_b64_tr_b16 a[104:105], v8 offset:7168\n\t"
        "v_exp_f32 v25, v25\n\t"
        "v_exp_f32 v26, v26\n\t"
        "v_exp_f32 v27, v27\n\t"
        "v_smfmac_f32_32x32x32_bf16 a[16:31], a[100:103], v[56:63], v9\n\t"
        "ds_read_b64_tr_b16 a[106:107], v8 offset:7392\n\t"
        "v_exp_f32 v28, v28\n\t"
        "v_exp_f32 v29, v29\n\t"
        "v_smfmac_f32_32x32x32_bf16 a[32:47], a[88:91], v[16:23], v9\n\t"
        "ds_read_b64_tr_b16 a[108:109], v8 offset:8064\n\t"
        "v_exp_f32 v30, v30\n\t"
        "v_exp_f32 v31, v31\n\t"
        "v_mfma_f32_32x32x16_bf16 v[48:63], v[116:119], a[68:71], 0\n\t"
        ".L_page1_%=:\n\t"
        "s_cbranch_vccnz .L_touched_%=\n\t"
        "ds_read_b64_tr_b16 a[110:111], v8 offset:8288\n\t"
        "v_exp_f32 v32, v32\n\t"
        "v_exp_f32 v33, v33\n\t"
        "v_exp_f32 v34, v34\n\t"
        "v_exp_f32 v35, v35\n\t"
        "v_pk_mul_f32 v[64:65], v[64:65], v[12:13] op_sel_hi:[1,0]\n\t"
        "v_pk_mul_f32 v[66:67], v[66:67], v[12:13] op_sel_hi:[1,0]\n\t"
        "v_pk_mul_f32 v[68:69], v[68:69], v[12:13] op_sel_hi:[1,0]\n\t"
        "v_pk_mul_f32 v[70:71], v[70:71], v[12:13] op_sel_hi:[1,0]\n\t"
        "v_mfma_f32_32x32x16_bf16 v[48:63], v[112:115], a[64:67], v[48:63]\n\t"
        "ds_read_b64_tr_b16 a[112:113], v8 offset:8960\n\t"
        "v_exp_f32 v36, v36\n\t"
        "v_exp_f32 v37, v37\n\t"
        "v_exp_f32 v38, v38\n\t"
        "v_exp_f32 v39, v39\n\t"
        "v_pk_mul_f32 v[80:81], v[80:81], v[12:13] op_sel_hi:[1,0]\n\t"
        "v_pk_mul_f32 v[82:83], v[82:83], v[12:13] op_sel_hi:[1,0]\n\t"
        "v_pk_mul_f32 v[84:85], v[84:85], v[12:13] op_sel_hi:[1,0]\n\t"
        "v_smfmac_f32_32x32x32_bf16 a[32:47], a[92:95], v[24:31], v9\n\t"
        "v_exp_f32 v40, v40\n\t"
        "v_exp_f32 v41, v41\n\t"
        "v_exp_f32 v42, v42\n\t"
        "v_exp_f32 v43, v43\n\t"
        "v_pk_mul_f32 v[72:73], v[72:73], v[12:13] op_sel:[0,1] op_sel_hi:[1,1]\n\t"
        "v_pk_mul_f32 v[74:75], v[74:75], v[12:13] op_sel:[0,1] op_sel_hi:[1,1]\n\t"
        "v_pk_mul_f32 v[76:77], v[76:77], v[12:13] op_sel:[0,1] op_sel_hi:[1,1]\n\t"
        "v_pk_mul_f32 v[78:79], v[78:79], v[12:13] op_sel:[0,1] op_sel_hi:[1,1]\n\t"
        "v_smfmac_f32_32x32x32_bf16 a[32:47], a[96:99], v[32:39], v9\n\t"
        "v_exp_f32 v44, v44\n\t"
        "v_exp_f32 v45, v45\n\t"
        "v_cvt_pk_bf16_f32 v226, v64, v65\n\t"
        "v_exp_f32 v46, v46\n\t"
        "v_cvt_pk_bf16_f32 v227, v66, v67\n\t"
        "v_exp_f32 v47, v47\n\t"
        "v_pk_mul_f32 v[88:89], v[88:89], v[12:13] op_sel:[0,1] op_sel_hi:[1,1]\n\t"
        "v_pk_mul_f32 v[90:91], v[90:91], v[12:13] op_sel:[0,1] op_sel_hi:[1,1]\n\t"
        "v_pk_mul_f32 v[92:93], v[92:93], v[12:13] op_sel:[0,1] op_sel_hi:[1,1]\n\t"
        "v_mfma_f32_32x32x16_bf16 v[16:31], v[124:127], a[68:71], 0\n\t"
        "v_exp_f32 v48, v48\n\t"
        "v_exp_f32 v49, v49\n\t"
        "v_cvt_pk_bf16_f32 v228, v68, v69\n\t"
        "v_exp_f32 v50, v50\n\t"
        "v_cvt_pk_bf16_f32 v229, v70, v71\n\t"
        "v_exp_f32 v51, v51\n\t"
        "v_cvt_pk_bf16_f32 v230, v80, v81\n\t"
        "v_mfma_f32_32x32x16_bf16 v[16:31], v[120:123], a[64:67], v[16:31]\n\t"
        "v_exp_f32 v52, v52\n\t"
        "v_exp_f32 v53, v53\n\t"
        "v_cvt_pk_bf16_f32 v231, v82, v83\n\t"
        "v_exp_f32 v54, v54\n\t"
        "v_cvt_pk_bf16_f32 v232, v84, v85\n\t"
        "v_exp_f32 v55, v55\n\t"
        "v_cvt_pk_bf16_f32 v234, v72, v73\n\t"
        "v_smfmac_f32_32x32x32_bf16 a[32:47], a[100:103], v[40:47], v9\n\t"
        "v_exp_f32 v56, v56\n\t"
        "v_exp_f32 v57, v57\n\t"
        "v_cvt_pk_bf16_f32 v235, v74, v75\n\t"
        "v_exp_f32 v58, v58\n\t"
        "v_cvt_pk_bf16_f32 v236, v76, v77\n\t"
        "v_exp_f32 v59, v59\n\t"
        "v_cvt_pk_bf16_f32 v237, v78, v79\n\t"
        "v_smfmac_f32_32x32x32_bf16 v[144:159], a[88:91], v[48:55], v9\n\t"
        "v_exp_f32 v60, v60\n\t"
        "v_exp_f32 v61, v61\n\t"
        "v_cvt_pk_bf16_f32 v238, v88, v89\n\t"
        "v_exp_f32 v62, v62\n\t"
        "v_cvt_pk_bf16_f32 v239, v90, v91\n\t"
        "v_exp_f32 v63, v63\n\t"
        "v_cvt_pk_bf16_f32 v240, v92, v93\n\t"
        "v_mfma_f32_32x32x16_bf16 v[32:47], v[230:233], v[164:167], 0\n\t"
        "ds_read_b64_tr_b16 a[114:115], v8 offset:9184\n\t"
        "v_exp_f32 v16, v16\n\t"
        "v_exp_f32 v17, v17\n\t"
        "v_exp_f32 v18, v18\n\t"
        "v_exp_f32 v19, v19\n\t"
        "v_mfma_f32_32x32x16_bf16 v[32:47], v[226:229], v[160:163], v[32:47]\n\t"
        "ds_read_b64_tr_b16 a[116:117], v8 offset:9856\n\t"
        "v_exp_f32 v20, v20\n\t"
        "v_exp_f32 v21, v21\n\t"
        "v_exp_f32 v22, v22\n\t"
        "v_exp_f32 v23, v23\n\t"
        "v_smfmac_f32_32x32x32_bf16 v[144:159], a[92:95], v[56:63], v9\n\t"
        "ds_read_b64_tr_b16 a[118:119], v8 offset:10080\n\t"
        "buffer_load_dword v223, v7, s[52:55], 0 offen sc1\n\t"
        "v_exp_f32 v24, v24\n\t"
        "v_exp_f32 v25, v25\n\t"
        "v_exp_f32 v26, v26\n\t"
        "v_exp_f32 v27, v27\n\t"
        "v_smfmac_f32_32x32x32_bf16 v[144:159], a[96:99], v[16:23], v9\n\t"
        "v_exp_f32 v28, v28\n\t"
        "v_exp_f32 v29, v29\n\t"
        "v_exp_f32 v30, v30\n\t"
        "v_exp_f32 v31, v31\n\t"
        "v_mfma_f32_32x32x16_bf16 v[48:63], v[238:241], v[164:167], 0\n\t"
        "v_exp_f32 v32, v32\n\t"
        "v_exp_f32 v33, v33\n\t"
        "v_exp_f32 v34, v34\n\t"
        "v_exp_f32 v35, v35\n\t"
        "v_mfma_f32_32x32x16_bf16 v[48:63], v[234:237], v[160:163], v[48:63]\n\t"
        "s_waitcnt lgkmcnt(0)\n\t"
        "v_exp_f32 v36, v36\n\t"
        "v_exp_f32 v37, v37\n\t"
        "v_exp_f32 v38, v38\n\t"
        "v_exp_f32 v39, v39\n\t"
        "v_smfmac_f32_32x32x32_bf16 v[144:159], a[100:103], v[24:31], v9\n\t"
        "v_exp_f32 v40, v40\n\t"
        "v_exp_f32 v41, v41\n\t"
        "v_exp_f32 v42, v42\n\t"
        "v_exp_f32 v43, v43\n\t"
        "v_smfmac_f32_32x32x32_bf16 a[0:15], a[104:107], v[32:39], v9\n\t"
        "v_exp_f32 v44, v44\n\t"
        "v_exp_f32 v45, v45\n\t"
        "v_exp_f32 v46, v46\n\t"
        "v_exp_f32 v47, v47\n\t"
        "v_mfma_f32_32x32x16_bf16 v[16:31], v[230:233], a[52:55], 0\n\t"
        "v_exp_f32 v48, v48\n\t"
        "v_exp_f32 v49, v49\n\t"
        "v_exp_f32 v50, v50\n\t"
        "v_exp_f32 v51, v51\n\t"
        "v_mfma_f32_32x32x16_bf16 v[16:31], v[226:229], a[48:51], v[16:31]\n\t"
        "v_exp_f32 v52, v52\n\t"
        "v_exp_f32 v53, v53\n\t"
        "v_exp_f32 v54, v54\n\t"
        "v_exp_f32 v55, v55\n\t"
        "v_smfmac_f32_32x32x32_bf16 a[0:15], a[108:111], v[40:47], v9\n\t"
        "s_movk_i32 s6, 4096\n\t"
        "v_exp_f32 v56, v56\n\t"
        "v_exp_f32 v57, v57\n\t"
        "v_exp_f32 v58, v58\n\t"
        "v_exp_f32 v59, v59\n\t"
        "v_smfmac_f32_32x32x32_bf16 a[0:15], a[112:115], v[48:55], v9\n\t"
        "s_add_u32 s4, s52, s56\n\t"
        "s_addc_u32 s5, s53, 0\n\t"
        "v_exp_f32 v60, v60\n\t"
        "v_exp_f32 v61, v61\n\t"
        "v_exp_f32 v62, v62\n\t"
        "v_exp_f32 v63, v63\n\t"
        "v_mfma_f32_32x32x16_bf16 v[32:47], v[238:241], a[52:55], 0\n\t"
        "s_waitcnt vmcnt(0)\n\t"
        "v_exp_f32 v16, v16\n\t"
        "v_exp_f32 v17, v17\n\t"
        "v_cmp_ne_u32 vcc, v223, v222\n\t"
        "v_exp_f32 v18, v18\n\t"
        "v_exp_f32 v19, v19\n\t"
        "v_mfma_f32_32x32x16_bf16 v[32:47], v[234:237], a[48:51], v[32:47]\n\t"
        "v_exp_f32 v20, v20\n\t"
        "v_exp_f32 v21, v21\n\t"
        "v_exp_f32 v22, v22\n\t"
        "v_exp_f32 v23, v23\n\t"
        "v_smfmac_f32_32x32x32_bf16 a[0:15], a[116:119], v[56:63], v9\n\t"
        "s_cbranch_vccnz .L_slow_%=\n\t"
        ".L_go_%=:\n\t"
        "buffer_load_dwordx4 v[96:99], v15, s[4:7], 0 offen offset:0 sc1\n\t"
        "buffer_load_dwordx4 v[100:103], v15, s[4:7], 0 offen offset:1024 sc1\n\t"
        "buffer_load_dwordx4 v[104:107], v15, s[4:7], 0 offen offset:2048 sc1\n\t"
        "buffer_load_dwordx4 v[108:111], v15, s[4:7], 0 offen offset:3072 sc1\n\t"
        "v_exp_f32 v24, v24\n\t"
        "v_exp_f32 v25, v25\n\t"
        "v_exp_f32 v26, v26\n\t"
        "v_exp_f32 v27, v27\n\t"
        "v_smfmac_f32_32x32x32_bf16 a[16:31], a[104:107], v[16:23], v9\n\t"
        "s_add_u32 s4, s4, 4096\n\t"
        "s_addc_u32 s5, s5, 0\n\t"
        "v_exp_f32 v28, v28\n\t"
        "v_exp_f32 v29, v29\n\t"
        "v_exp_f32 v30, v30\n\t"
        "v_exp_f32 v31, v31\n\t"
        "v_mfma_f32_32x32x16_bf16 v[48:63], v[230:233], a[60:63], 0\n\t"
        "v_exp_f32 v32, v32\n\t"
        "v_exp_f32 v33, v33\n\t"
        "v_exp_f32 v34, v34\n\t"
        "v_exp_f32 v35, v35\n\t"
        "v_mfma_f32_32x32x16_bf16 v[48:63], v[226:229], a[56:59], v[48:63]\n\t"
        "v_exp_f32 v36, v36\n\t"
        "v_exp_f32 v37, v37\n\t"
        "v_exp_f32 v38, v38\n\t"
        "v_exp_f32 v39, v39\n\t"
        "v_smfmac_f32_32x32x32_bf16 a[16:31], a[108:111], v[24:31], v9\n\t"
        "v_exp_f32 v40, v40\n\t"
        "v_exp_f32 v41, v41\n\t"
        "v_exp_f32 v42, v42\n\t"
        "v_exp_f32 v43, v43\n\t"
        "v_smfmac_f32_32x32x32_bf16 a[16:31], a[112:115], v[32:39], v9\n\t"
        "v_exp_f32 v44, v44\n\t"
        "v_exp_f32 v45, v45\n\t"
        "v_exp_f32 v46, v46\n\t"
        "v_exp_f32 v47, v47\n\t"
        "v_mfma_f32_32x32x16_bf16 v[16:31], v[238:241], a[60:63], 0\n\t"
        "s_waitcnt vmcnt(7)\n\t"
        "v_exp_f32 v48, v48\n\t"
        "v_exp_f32 v49, v49\n\t"
        "v_exp_f32 v50, v50\n\t"
        "v_exp_f32 v51, v51\n\t"
        "v_mfma_f32_32x32x16_bf16 v[16:31], v[234:237], a[56:59], v[16:31]\n\t"
        "s_addk_i32 m0, 4096\n\t"
        "ds_read_b64_tr_b16 a[72:73], v8 offset:10752\n\t"
        "v_exp_f32 v52, v52\n\t"
        "v_exp_f32 v53, v53\n\t"
        "v_exp_f32 v54, v54\n\t"
        "v_exp_f32 v55, v55\n\t"
        "v_smfmac_f32_32x32x32_bf16 a[16:31], a[116:119], v[40:47], v9\n\t"
        "ds_read_b64_tr_b16 a[74:75], v8 offset:10976\n\t"
        "buffer_load_dwordx4 v15, s[28:31], m0 offen offset:0 lds\n\t"
        "buffer_load_dwordx4 v15, s[28:31], m0 offen offset:1024 lds\n\t"
        "buffer_load_dwordx4 v15, s[28:31], m0 offen offset:2048 lds\n\t"
        "buffer_load_dwordx4 v15, s[28:31], m0 offen offset:3072 lds\n\t"
        "v_exp_f32 v56, v56\n\t"
        "v_exp_f32 v57, v57\n\t"
        "v_exp_f32 v58, v58\n\t"
        "v_exp_f32 v59, v59\n\t"
        "v_smfmac_f32_32x32x32_bf16 a[32:47], a[104:107], v[48:55], v9\n\t"
        "ds_read_b64_tr_b16 a[76:77], v8 offset:11648\n\t"
        "v_exp_f32 v60, v60\n\t"
        "v_exp_f32 v61, v61\n\t"
        "v_exp_f32 v62, v62\n\t"
        "v_exp_f32 v63, v63\n\t"
        "v_mfma_f32_32x32x16_bf16 v[32:47], v[230:233], a[68:71], 0\n\t"
        "ds_read_b64_tr_b16 a[78:79], v8 offset:11872\n\t"
        "v_exp_f32 v16, v16\n\t"
        "v_exp_f32 v17, v17\n\t"
        "v_exp_f32 v18, v18\n\t"
        "v_exp_f32 v19, v19\n\t"
        "v_mfma_f32_32x32x16_bf16 v[32:47], v[226:229], a[64:67], v[32:47]\n\t"
        "ds_read_b64_tr_b16 a[80:81], v8 offset:12544\n\t"
        "v_exp_f32 v20, v20\n\t"
        "v_exp_f32 v21, v21\n\t"
        "v_exp_f32 v22, v22\n\t"
        "v_exp_f32 v23, v23\n\t"
        "v_smfmac_f32_32x32x32_bf16 a[32:47], a[108:111], v[56:63], v9\n\t"
        "v_exp_f32 v24, v24\n\t"
        "v_exp_f32 v25, v25\n\t"
        "v_exp_f32 v26, v26\n\t"
        "v_exp_f32 v27, v27\n\t"
        "v_smfmac_f32_32x32x32_bf16 a[32:47], a[112:115], v[16:23], v9\n\t"
        "v_exp_f32 v28, v28\n\t"
        "v_exp_f32 v29, v29\n\t"
        "v_exp_f32 v30, v30\n\t"
        "v_exp_f32 v31, v31\n\t"
        "v_mfma_f32_32x32x16_bf16 v[48:63], v[238:241], a[68:71], 0\n\t"
        "v_exp_f32 v32, v32\n\t"
        "v_exp_f32 v33, v33\n\t"
        "v_exp_f32 v34, v34\n\t"
        "v_exp_f32 v35, v35\n\t"
        "v_mfma_f32_32x32x16_bf16 v[48:63], v[234:237], a[64:67], v[48:63]\n\t"
        "v_exp_f32 v36, v36\n\t"
        "v_exp_f32 v37, v37\n\t"
        "v_exp_f32 v38, v38\n\t"
        "v_exp_f32 v39, v39\n\t"
        "v_smfmac_f32_32x32x32_bf16 a[32:47], a[116:119], v[24:31], v9\n\t"
        "buffer_load_dwordx4 v[112:115], v15, s[4:7], 0 offen offset:0 sc1\n\t"
        "buffer_load_dwordx4 v[116:119], v15, s[4:7], 0 offen offset:1024 sc1\n\t"
        "buffer_load_dwordx4 v[120:123], v15, s[4:7], 0 offen offset:2048 sc1\n\t"
        "buffer_load_dwordx4 v[124:127], v15, s[4:7], 0 offen offset:3072 sc1\n\t"
        "v_exp_f32 v40, v40\n\t"
        "v_exp_f32 v41, v41\n\t"
        "v_exp_f32 v42, v42\n\t"
        "v_exp_f32 v43, v43\n\t"
        "v_smfmac_f32_32x32x32_bf16 v[144:159], a[104:107], v[32:39], v9\n\t"
        "s_waitcnt vmcnt(8)\n\t"
        "s_add_u32 s4, s4, 4096\n\t"
        "s_addc_u32 s5, s5, 0\n\t"
        "v_exp_f32 v44, v44\n\t"
        "v_exp_f32 v45, v45\n\t"
        "v_exp_f32 v46, v46\n\t"
        "v_exp_f32 v47, v47\n\t"
        "v_mfma_f32_32x32x16_bf16 v[16:31], v[100:103], v[164:167], 0\n\t"
        "ds_read_b64_tr_b16 a[82:83], v8 offset:12768\n\t"
        "v_exp_f32 v48, v48\n\t"
        "v_exp_f32 v49, v49\n\t"
        "v_exp_f32 v50, v50\n\t"
        "v_exp_f32 v51, v51\n\t"
        "v_mfma_f32_32x32x16_bf16 v[16:31], v[96:99], v[160:163], v[16:31]\n\t"
        "ds_read_b64_tr_b16 a[84:85], v8 offset:13440\n\t"
        "v_exp_f32 v52, v52\n\t"
        "v_exp_f32 v53, v53\n\t"
        "v_exp_f32 v54, v54\n\t"
        "v_exp_f32 v55, v55\n\t"
        "v_smfmac_f32_32x32x32_bf16 v[144:159], a[108:111], v[40:47], v9\n\t"
        "ds_read_b64_tr_b16 a[86:87], v8 offset:13664\n\t"
        "v_exp_f32 v56, v56\n\t"
        "v_exp_f32 v57, v57\n\t"
        "v_exp_f32 v58, v58\n\t"
        "v_exp_f32 v59, v59\n\t"
        "v_smfmac_f32_32x32x32_bf16 v[144:159], a[112:115], v[48:55], v9\n\t"
        "v_exp_f32 v60, v60\n\t"
        "v_exp_f32 v61, v61\n\t"
        "v_exp_f32 v62, v62\n\t"
        "v_exp_f32 v63, v63\n\t"
        "v_mfma_f32_32x32x16_bf16 v[32:47], v[108:111], v[164:167], 0\n\t"
        "v_exp_f32 v16, v16\n\t"
        "v_exp_f32 v17, v17\n\t"
        "v_exp_f32 v18, v18\n\t"
        "v_exp_f32 v19, v19\n\t"
        "v_mfma_f32_32x32x16_bf16 v[32:47], v[104:107], v[160:163], v[32:47]\n\t"
        "s_waitcnt lgkmcnt(0)\n\t"
        "v_exp_f32 v20, v20\n\t"
        "v_exp_f32 v21, v21\n\t"
        "v_exp_f32 v22, v22\n\t"
        "v_exp_f32 v23, v23\n\t"
        "v_smfmac_f32_32x32x32_bf16 v[144:159], a[116:119], v[56:63], v9\n\t"
        "v_exp_f32 v24, v24\n\t"
        "v_exp_f32 v25, v25\n\t"
        "v_exp_f32 v26, v26\n\t"
        "v_exp_f32 v27, v27\n\t"
        "v_smfmac_f32_32x32x32_bf16 a[0:15], a[72:75], v[16:23], v9\n\t"
        "v_exp_f32 v28, v28\n\t"
        "v_exp_f32 v29, v29\n\t"
        "v_exp_f32 v30, v30\n\t"
        "v_exp_f32 v31, v31\n\t"
        "v_mfma_f32_32x32x16_bf16 v[48:63], v[100:103], a[52:55], 0\n\t"
        "v_exp_f32 v32, v32\n\t"
        "v_exp_f32 v33, v33\n\t"
        "v_exp_f32 v34, v34\n\t"
        "v_exp_f32 v35, v35\n\t"
        "v_mfma_f32_32x32x16_bf16 v[48:63], v[96:99], a[48:51], v[48:63]\n\t"
        "s_addk_i32 m0, 4096\n\t"
        "v_exp_f32 v36, v36\n\t"
        "v_exp_f32 v37, v37\n\t"
        "v_exp_f32 v38, v38\n\t"
        "v_exp_f32 v39, v39\n\t"
        "v_smfmac_f32_32x32x32_bf16 a[0:15], a[76:79], v[24:31], v9\n\t"
        "buffer_load_dwordx4 v15, s[28:31], m0 offen offset:0 lds\n\t"
        "buffer_load_dwordx4 v15, s[28:31], m0 offen offset:1024 lds\n\t"
        "buffer_load_dwordx4 v15, s[28:31], m0 offen offset:2048 lds\n\t"
        "buffer_load_dwordx4 v15, s[28:31], m0 offen offset:3072 lds\n\t"
        "v_exp_f32 v40, v40\n\t"
        "v_exp_f32 v41, v41\n\t"
        "v_exp_f32 v42, v42\n\t"
        "v_exp_f32 v43, v43\n\t"
        "v_smfmac_f32_32x32x32_bf16 a[0:15], a[80:83], v[32:39], v9\n\t"
        "v_exp_f32 v44, v44\n\t"
        "v_exp_f32 v45, v45\n\t"
        "v_exp_f32 v46, v46\n\t"
        "v_exp_f32 v47, v47\n\t"
        "v_mfma_f32_32x32x16_bf16 v[16:31], v[108:111], a[52:55], 0\n\t"
        "v_exp_f32 v48, v48\n\t"
        "v_exp_f32 v49, v49\n\t"
        "v_exp_f32 v50, v50\n\t"
        "v_exp_f32 v51, v51\n\t"
        "v_mfma_f32_32x32x16_bf16 v[16:31], v[104:107], a[48:51], v[16:31]\n\t"
        "v_exp_f32 v52, v52\n\t"
        "v_exp_f32 v53, v53\n\t"
        "v_exp_f32 v54, v54\n\t"
        "v_exp_f32 v55, v55\n\t"
        "v_smfmac_f32_32x32x32_bf16 a[0:15], a[84:87], v[40:47], v9\n\t"
        "v_exp_f32 v56, v56\n\t"
        "v_exp_f32 v57, v57\n\t"
        "v_exp_f32 v58, v58\n\t"
        "v_exp_f32 v59, v59\n\t"
        "v_smfmac_f32_32x32x32_bf16 a[16:31], a[72:75], v[48:55], v9\n\t"
        "v_exp_f32 v60, v60\n\t"
        "v_exp_f32 v61, v61\n\t"
        "v_exp_f32 v62, v62\n\t"
        "v_exp_f32 v63, v63\n\t"
        "v_mfma_f32_32x32x16_bf16 v[32:47], v[100:103], a[60:63], 0\n\t"
        "v_exp_f32 v16, v16\n\t"
        "v_exp_f32 v17, v17\n\t"
        "v_exp_f32 v18, v18\n\t"
        "v_exp_f32 v19, v19\n\t"
        "v_mfma_f32_32x32x16_bf16 v[32:47], v[96:99], a[56:59], v[32:47]\n\t"
        "v_exp_f32 v20, v20\n\t"
        "v_exp_f32 v21, v21\n\t"
        "v_exp_f32 v22, v22\n\t"
        "v_exp_f32 v23, v23\n\t"
        "v_smfmac_f32_32x32x32_bf16 a[16:31], a[76:79], v[56:63], v9\n\t"
        "v_exp_f32 v24, v24\n\t"
        "v_exp_f32 v25, v25\n\t"
        "v_exp_f32 v26, v26\n\t"
        "v_exp_f32 v27, v27\n\t"
        "v_smfmac_f32_32x32x32_bf16 a[16:31], a[80:83], v[16:23], v9\n\t"
        "v_exp_f32 v28, v28\n\t"
        "v_exp_f32 v29, v29\n\t"
        "v_exp_f32 v30, v30\n\t"
        "v_exp_f32 v31, v31\n\t"
        "v_mfma_f32_32x32x16_bf16 v[48:63], v[108:111], a[60:63], 0\n\t"
        "s_waitcnt vmcnt(10)\n\t"
        "v_exp_f32 v32, v32\n\t"
        "v_exp_f32 v33, v33\n\t"
        "v_exp_f32 v34, v34\n\t"
        "v_exp_f32 v35, v35\n\t"
        "v_mfma_f32_32x32x16_bf16 v[48:63], v[104:107], a[56:59], v[48:63]\n\t"
        "ds_read_b64_tr_b16 a[88:89], v8 offset:14336\n\t"
        "v_exp_f32 v36, v36\n\t"
        "v_exp_f32 v37, v37\n\t"
        "v_exp_f32 v38, v38\n\t"
        "v_exp_f32 v39, v39\n\t"
        "v_smfmac_f32_32x32x32_bf16 a[16:31], a[84:87], v[24:31], v9\n\t"
        "ds_read_b64_tr_b16 a[90:91], v8 offset:14560\n\t"
        "buffer_load_dwordx4 v[226:229], v15, s[4:7], 0 offen offset:0 sc1\n\t"
        "buffer_load_dwordx4 v[230:233], v15, s[4:7], 0 offen offset:1024 sc1\n\t"
        "buffer_load_dwordx4 v[234:237], v15, s[4:7], 0 offen offset:2048 sc1\n\t"
        "buffer_load_dwordx4 v[238:241], v15, s[4:7], 0 offen offset:3072 sc1\n\t"
        "v_exp_f32 v40, v40\n\t"
        "v_exp_f32 v41, v41\n\t"
        "v_exp_f32 v42, v42\n\t"
        "v_exp_f32 v43, v43\n\t"
        "v_smfmac_f32_32x32x32_bf16 a[32:47], a[72:75], v[32:39], v9\n\t"
        "s_add_u32 s4, s4, 4096\n\t"
        "s_addc_u32 s5, s5, 0\n\t"
        "ds_read_b64_tr_b16 a[92:93], v8 offset:15232\n\t"
        "v_exp_f32 v44, v44\n\t"
        "v_exp_f32 v45, v45\n\t"
        "v_exp_f32 v46, v46\n\t"
        "v_exp_f32 v47, v47\n\t"
        "v_mfma_f32_32x32x16_bf16 v[16:31], v[100:103], a[68:71], 0\n\t"
        "ds_read_b64_tr_b16 a[94:95], v8 offset:15456\n\t"
        "v_exp_f32 v48, v48\n\t"
        "v_exp_f32 v49, v49\n\t"
        "v_exp_f32 v50, v50\n\t"
        "v_exp_f32 v51, v51\n\t"
        "v_mfma_f32_32x32x16_bf16 v[16:31], v[96:99], a[64:67], v[16:31]\n\t"
        "ds_read_b64_tr_b16 a[96:97], v8 offset:16128\n\t"
        "v_exp_f32 v52, v52\n\t"
        "v_exp_f32 v53, v53\n\t"
        "v_exp_f32 v54, v54\n\t"
        "v_exp_f32 v55, v55\n\t"
        "v_smfmac_f32_32x32x32_bf16 a[32:47], a[76:79], v[40:47], v9\n\t"
        "v_exp_f32 v56, v56\n\t"
        "v_exp_f32 v57, v57\n\t"
        "v_exp_f32 v58, v58\n\t"
        "v_exp_f32 v59, v59\n\t"
        "v_smfmac_f32_32x32x32_bf16 a[32:47], a[80:83], v[48:55], v9\n\t"
        "v_exp_f32 v60, v60\n\t"
        "v_exp_f32 v61, v61\n\t"
        "v_exp_f32 v62, v62\n\t"
        "v_exp_f32 v63, v63\n\t"
        "v_mfma_f32_32x32x16_bf16 v[32:47], v[108:111], a[68:71], 0\n\t"
        "v_exp_f32 v16, v16\n\t"
        "v_exp_f32 v17, v17\n\t"
        "v_exp_f32 v18, v18\n\t"
        "v_exp_f32 v19, v19\n\t"
        "v_mfma_f32_32x32x16_bf16 v[32:47], v[104:107], a[64:67], v[32:47]\n\t"
        "v_exp_f32 v20, v20\n\t"
        "v_exp_f32 v21, v21\n\t"
        "v_exp_f32 v22, v22\n\t"
        "v_exp_f32 v23, v23\n\t"
        "v_smfmac_f32_32x32x32_bf16 a[32:47], a[84:87], v[56:63], v9\n\t"
        "v_exp_f32 v24, v24\n\t"
        "v_exp_f32 v25, v25\n\t"
        "v_exp_f32 v26, v26\n\t"
        "v_exp_f32 v27, v27\n\t"
        "v_smfmac_f32_32x32x32_bf16 v[144:159], a[72:75], v[16:23], v9\n\t"
        "s_waitcnt vmcnt(8)\n\t"
        "v_exp_f32 v28, v28\n\t"
        "v_exp_f32 v29, v29\n\t"
        "v_exp_f32 v30, v30\n\t"
        "v_exp_f32 v31, v31\n\t"
        "v_mfma_f32_32x32x16_bf16 v[48:63], v[116:119], v[164:167], 0\n\t"
        "ds_read_b64_tr_b16 a[98:99], v8 offset:16352\n\t"
        "v_exp_f32 v32, v32\n\t"
        "v_exp_f32 v33, v33\n\t"
        "v_exp_f32 v34, v34\n\t"
        "v_exp_f32 v35, v35\n\t"
        "v_mfma_f32_32x32x16_bf16 v[48:63], v[112:115], v[160:163], v[48:63]\n\t"
        "s_addk_i32 m0, 4096\n\t"
        "ds_read_b64_tr_b16 a[100:101], v8 offset:17024\n\t"
        "v_exp_f32 v36, v36\n\t"
        "v_exp_f32 v37, v37\n\t"
        "v_exp_f32 v38, v38\n\t"
        "v_exp_f32 v39, v39\n\t"
        "v_smfmac_f32_32x32x32_bf16 v[144:159], a[76:79], v[24:31], v9\n\t"
        "ds_read_b64_tr_b16 a[102:103], v8 offset:17248\n\t"
        "buffer_load_dwordx4 v15, s[28:31], m0 offen offset:0 lds\n\t"
        "buffer_load_dwordx4 v15, s[28:31], m0 offen offset:1024 lds\n\t"
        "buffer_load_dwordx4 v15, s[28:31], m0 offen offset:2048 lds\n\t"
        "buffer_load_dwordx4 v15, s[28:31], m0 offen offset:3072 lds\n\t"
        "v_exp_f32 v40, v40\n\t"
        "v_exp_f32 v41, v41\n\t"
        "v_exp_f32 v42, v42\n\t"
        "v_exp_f32 v43, v43\n\t"
        "v_smfmac_f32_32x32x32_bf16 v[144:159], a[80:83], v[32:39], v9\n\t"
        "v_exp_f32 v44, v44\n\t"
        "v_exp_f32 v45, v45\n\t"
        "v_exp_f32 v46, v46\n\t"
        "v_exp_f32 v47, v47\n\t"
        "v_mfma_f32_32x32x16_bf16 v[16:31], v[124:127], v[164:167], 0\n\t"
        "v_exp_f32 v48, v48\n\t"
        "v_exp_f32 v49, v49\n\t"
        "v_exp_f32 v50, v50\n\t"
        "v_exp_f32 v51, v51\n\t"
        "v_mfma_f32_32x32x16_bf16 v[16:31], v[120:123], v[160:163], v[16:31]\n\t"
        "s_waitcnt lgkmcnt(0)\n\t"
        "v_exp_f32 v52, v52\n\t"
        "v_exp_f32 v53, v53\n\t"
        "v_exp_f32 v54, v54\n\t"
        "v_exp_f32 v55, v55\n\t"
        "v_smfmac_f32_32x32x32_bf16 v[144:159], a[84:87], v[40:47], v9\n\t"
        "v_exp_f32 v56, v56\n\t"
        "v_exp_f32 v57, v57\n\t"
        "v_exp_f32 v58, v58\n\t"
        "v_exp_f32 v59, v59\n\t"
        "v_smfmac_f32_32x32x32_bf16 a[0:15], a[88:91], v[48:55], v9\n\t"
        "v_exp_f32 v60, v60\n\t"
        "v_exp_f32 v61, v61\n\t"
        "v_exp_f32 v62, v62\n\t"
        "v_exp_f32 v63, v63\n\t"
        "v_mfma_f32_32x32x16_bf16 v[32:47], v[116:119], a[52:55], 0\n\t"
        "v_exp_f32 v16, v16\n\t"
        "v_exp_f32 v17, v17\n\t"
        "v_exp_f32 v18, v18\n\t"
        "v_exp_f32 v19, v19\n\t"
        "v_mfma_f32_32x32x16_bf16 v[32:47], v[112:115], a[48:51], v[32:47]\n\t"
        "v_exp_f32 v20, v20\n\t"
        "v_exp_f32 v21, v21\n\t"
        "v_exp_f32 v22, v22\n\t"
        "v_exp_f32 v23, v23\n\t"
        "v_smfmac_f32_32x32x32_bf16 a[0:15], a[92:95], v[56:63], v9\n\t"
        "v_exp_f32 v24, v24\n\t"
        "v_exp_f32 v25, v25\n\t"
        "v_exp_f32 v26, v26\n\t"
        "v_exp_f32 v27, v27\n\t"
        "v_smfmac_f32_32x32x32_bf16 a[0:15], a[96:99], v[16:23], v9\n\t"
        "v_exp_f32 v28, v28\n\t"
        "v_exp_f32 v29, v29\n\t"
        "v_exp_f32 v30, v30\n\t"
        "v_exp_f32 v31, v31\n\t"
        "v_mfma_f32_32x32x16_bf16 v[48:63], v[124:127], a[52:55], 0\n\t"
        "v_exp_f32 v32, v32\n\t"
        "v_exp_f32 v33, v33\n\t"
        "v_exp_f32 v34, v34\n\t"
        "v_exp_f32 v35, v35\n\t"
        "v_mfma_f32_32x32x16_bf16 v[48:63], v[120:123], a[48:51], v[48:63]\n\t"
        "v_exp_f32 v36, v36\n\t"
        "v_exp_f32 v37, v37\n\t"
        "v_exp_f32 v38, v38\n\t"
        "v_exp_f32 v39, v39\n\t"
        "v_smfmac_f32_32x32x32_bf16 a[0:15], a[100:103], v[24:31], v9\n\t"
        "buffer_load_dwordx4 v[96:99], v15, s[4:7], 0 offen offset:0 sc1\n\t"
        "buffer_load_dwordx4 v[100:103], v15, s[4:7], 0 offen offset:1024 sc1\n\t"
        "buffer_load_dwordx4 v[104:107], v15, s[4:7], 0 offen offset:2048 sc1\n\t"
        "buffer_load_dwordx4 v[108:111], v15, s[4:7], 0 offen offset:3072 sc1\n\t"
        "v_exp_f32 v40, v40\n\t"
        "v_exp_f32 v41, v41\n\t"
        "v_exp_f32 v42, v42\n\t"
        "v_exp_f32 v43, v43\n\t"
        "v_smfmac_f32_32x32x32_bf16 a[16:31], a[88:91], v[32:39], v9\n\t"
        "s_add_u32 s4, s4, 4096\n\t"
        "s_addc_u32 s5, s5, 0\n\t"
        "v_exp_f32 v44, v44\n\t"
        "v_exp_f32 v45, v45\n\t"
        "v_exp_f32 v46, v46\n\t"
        "v_exp_f32 v47, v47\n\t"
        "v_mfma_f32_32x32x16_bf16 v[16:31], v[116:119], a[60:63], 0\n\t"
        "v_exp_f32 v48, v48\n\t"
        "v_exp_f32 v49, v49\n\t"
        "v_exp_f32 v50, v50\n\t"
        "v_exp_f32 v51, v51\n\t"
        "v_mfma_f32_32x32x16_bf16 v[16:31], v[112:115], a[56:59], v[16:31]\n\t"
        "v_exp_f32 v52, v52\n\t"
        "v_exp_f32 v53, v53\n\t"
        "v_exp_f32 v54, v54\n\t"
        "v_exp_f32 v55, v55\n\t"
        "v_smfmac_f32_32x32x32_bf16 a[16:31], a[92:95], v[40:47], v9\n\t"
        "v_exp_f32 v56, v56\n\t"
        "v_exp_f32 v57, v57\n\t"
        "v_exp_f32 v58, v58\n\t"
        "v_exp_f32 v59, v59\n\t"
        "v_smfmac_f32_32x32x32_bf16 a[16:31], a[96:99], v[48:55], v9\n\t"
        "v_exp_f32 v60, v60\n\t"
        "v_exp_f32 v61, v61\n\t"
        "v_exp_f32 v62, v62\n\t"
        "v_exp_f32 v63, v63\n\t"
        "v_mfma_f32_32x32x16_bf16 v[32:47], v[124:127], a[60:63], 0\n\t"
        "s_waitcnt vmcnt(15)\n\t"
        "v_exp_f32 v16, v16\n\t"
        "v_exp_f32 v17, v17\n\t"
        "v_exp_f32 v18, v18\n\t"
        "v_exp_f32 v19, v19\n\t"
        "v_mfma_f32_32x32x16_bf16 v[32:47], v[120:123], a[56:59], v[32:47]\n\t"
        "ds_read_b64_tr_b16 a[104:105], v8 offset:17920\n\t"
        "v_exp_f32 v20, v20\n\t"
        "v_exp_f32 v21, v21\n\t"
        "v_exp_f32 v22, v22\n\t"
        "v_exp_f32 v23, v23\n\t"
        "v_smfmac_f32_32x32x32_bf16 a[16:31], a[100:103], v[56:63], v9\n\t"
        "ds_read_b64_tr_b16 a[106:107], v8 offset:18144\n\t"
        "v_exp_f32 v24, v24\n\t"
        "v_exp_f32 v25, v25\n\t"
        "v_exp_f32 v26, v26\n\t"
        "v_exp_f32 v27, v27\n\t"
        "v_smfmac_f32_32x32x32_bf16 a[32:47], a[88:91], v[16:23], v9\n\t"
        "ds_read_b64_tr_b16 a[108:109], v8 offset:18816\n\t"
        "v_exp_f32 v28, v28\n\t"
        "v_exp_f32 v29, v29\n\t"
        "v_exp_f32 v30, v30\n\t"
        "v_exp_f32 v31, v31\n\t"
        "v_mfma_f32_32x32x16_bf16 v[48:63], v[116:119], a[68:71], 0\n\t"
        "ds_read_b64_tr_b16 a[110:111], v8 offset:19040\n\t"
        "v_exp_f32 v32, v32\n\t"
        "v_exp_f32 v33, v33\n\t"
        "v_exp_f32 v34, v34\n\t"
        "v_exp_f32 v35, v35\n\t"
        "v_mfma_f32_32x32x16_bf16 v[48:63], v[112:115], a[64:67], v[48:63]\n\t"
        "ds_read_b64_tr_b16 a[112:113], v8 offset:19712\n\t"
        "v_exp_f32 v36, v36\n\t"
        "v_exp_f32 v37, v37\n\t"
        "v_exp_f32 v38, v38\n\t"
        "v_exp_f32 v39, v39\n\t"
        "v_smfmac_f32_32x32x32_bf16 a[32:47], a[92:95], v[24:31], v9\n\t"
        "v_exp_f32 v40, v40\n\t"
        "v_exp_f32 v41, v41\n\t"
        "v_exp_f32 v42, v42\n\t"
        "v_exp_f32 v43, v43\n\t"
        "v_smfmac_f32_32x32x32_bf16 a[32:47], a[96:99], v[32:39], v9\n\t"
        "v_exp_f32 v44, v44\n\t"
        "v_exp_f32 v45, v45\n\t"
        "v_exp_f32 v46, v46\n\t"
        "v_exp_f32 v47, v47\n\t"
        "v_mfma_f32_32x32x16_bf16 v[16:31], v[124:127], a[68:71], 0\n\t"
        "v_exp_f32 v48, v48\n\t"
        "v_exp_f32 v49, v49\n\t"
        "v_exp_f32 v50, v50\n\t"
        "v_exp_f32 v51, v51\n\t"
        "v_mfma_f32_32x32x16_bf16 v[16:31], v[120:123], a[64:67], v[16:31]\n\t"
        "v_exp_f32 v52, v52\n\t"
        "v_exp_f32 v53, v53\n\t"
        "v_exp_f32 v54, v54\n\t"
        "v_exp_f32 v55, v55\n\t"
        "v_smfmac_f32_32x32x32_bf16 a[32:47], a[100:103], v[40:47], v9\n\t"
        "v_exp_f32 v56, v56\n\t"
        "v_exp_f32 v57, v57\n\t"
        "v_exp_f32 v58, v58\n\t"
        "v_exp_f32 v59, v59\n\t"
        "v_smfmac_f32_32x32x32_bf16 v[144:159], a[88:91], v[48:55], v9\n\t"
        "s_waitcnt vmcnt(8)\n\t"
        "v_exp_f32 v60, v60\n\t"
        "v_exp_f32 v61, v61\n\t"
        "v_exp_f32 v62, v62\n\t"
        "v_exp_f32 v63, v63\n\t"
        "v_mfma_f32_32x32x16_bf16 v[32:47], v[230:233], v[164:167], 0\n\t"
        "ds_read_b64_tr_b16 a[114:115], v8 offset:19936\n\t"
        "v_exp_f32 v16, v16\n\t"
        "v_exp_f32 v17, v17\n\t"
        "v_exp_f32 v18, v18\n\t"
        "v_exp_f32 v19, v19\n\t"
        "v_mfma_f32_32x32x16_bf16 v[32:47], v[226:229], v[160:163], v[32:47]\n\t"
        "ds_read_b64_tr_b16 a[116:117], v8 offset:20608\n\t"
        "v_exp_f32 v20, v20\n\t"
        "v_exp_f32 v21, v21\n\t"
        "v_exp_f32 v22, v22\n\t"
        "v_exp_f32 v23, v23\n\t"
        "v_smfmac_f32_32x32x32_bf16 v[144:159], a[92:95], v[56:63], v9\n\t"
        "ds_read_b64_tr_b16 a[118:119], v8 offset:20832\n\t"
        "v_exp_f32 v24, v24\n\t"
        "v_exp_f32 v25, v25\n\t"
        "v_exp_f32 v26, v26\n\t"
        "v_exp_f32 v27, v27\n\t"
        "v_smfmac_f32_32x32x32_bf16 v[144:159], a[96:99], v[16:23], v9\n\t"
        "v_exp_f32 v28, v28\n\t"
        "v_exp_f32 v29, v29\n\t"
        "v_exp_f32 v30, v30\n\t"
        "v_exp_f32 v31, v31\n\t"
        "v_mfma_f32_32x32x16_bf16 v[48:63], v[238:241], v[164:167], 0\n\t"
        "v_exp_f32 v32, v32\n\t"
        "v_exp_f32 v33, v33\n\t"
        "v_exp_f32 v34, v34\n\t"
        "v_exp_f32 v35, v35\n\t"
        "v_mfma_f32_32x32x16_bf16 v[48:63], v[234:237], v[160:163], v[48:63]\n\t"
        "s_waitcnt lgkmcnt(0)\n\t"
        "v_exp_f32 v36, v36\n\t"
        "v_exp_f32 v37, v37\n\t"
        "v_exp_f32 v38, v38\n\t"
        "v_exp_f32 v39, v39\n\t"
        "v_smfmac_f32_32x32x32_bf16 v[144:159], a[100:103], v[24:31], v9\n\t"
        "buffer_load_dwordx4 v[112:115], v15, s[4:7], 0 offen offset:0 sc1\n\t"
        "buffer_load_dwordx4 v[116:119], v15, s[4:7], 0 offen offset:1024 sc1\n\t"
        "buffer_load_dwordx4 v[120:123], v15, s[4:7], 0 offen offset:2048 sc1\n\t"
        "buffer_load_dwordx4 v[124:127], v15, s[4:7], 0 offen offset:3072 sc1\n\t"
        "v_exp_f32 v40, v40\n\t"
        "v_exp_f32 v41, v41\n\t"
        "v_exp_f32 v42, v42\n\t"
        "v_exp_f32 v43, v43\n\t"
        "v_smfmac_f32_32x32x32_bf16 a[0:15], a[104:107], v[32:39], v9\n\t"
        "v_exp_f32 v44, v44\n\t"
        "v_exp_f32 v45, v45\n\t"
        "v_exp_f32 v46, v46\n\t"
        "v_exp_f32 v47, v47\n\t"
        "v_mfma_f32_32x32x16_bf16 v[16:31], v[230:233], a[52:55], 0\n\t"
        "v_exp_f32 v48, v48\n\t"
        "v_exp_f32 v49, v49\n\t"
        "v_exp_f32 v50, v50\n\t"
        "v_exp_f32 v51, v51\n\t"
        "v_mfma_f32_32x32x16_bf16 v[16:31], v[226:229], a[48:51], v[16:31]\n\t"
        "v_exp_f32 v52, v52\n\t"
        "v_exp_f32 v53, v53\n\t"
        "v_exp_f32 v54, v54\n\t"
        "v_exp_f32 v55, v55\n\t"
        "v_smfmac_f32_32x32x32_bf16 a[0:15], a[108:111], v[40:47], v9\n\t"
        "v_exp_f32 v56, v56\n\t"
        "v_exp_f32 v57, v57\n\t"
        "v_exp_f32 v58, v58\n\t"
        "v_exp_f32 v59, v59\n\t"
        "v_smfmac_f32_32x32x32_bf16 a[0:15], a[112:115], v[48:55], v9\n\t"
        "v_exp_f32 v60, v60\n\t"
        "v_exp_f32 v61, v61\n\t"
        "v_exp_f32 v62, v62\n\t"
        "v_exp_f32 v63, v63\n\t"
        "v_mfma_f32_32x32x16_bf16 v[32:47], v[238:241], a[52:55], 0\n\t"
        "s_waitcnt vmcnt(11)\n\t"
        "v_exp_f32 v16, v16\n\t"
        "v_exp_f32 v17, v17\n\t"
        "v_exp_f32 v18, v18\n\t"
        "v_exp_f32 v19, v19\n\t"
        "v_mfma_f32_32x32x16_bf16 v[32:47], v[234:237], a[48:51], v[32:47]\n\t"
        "ds_read_b64_tr_b16 a[72:73], v8 offset:21504\n\t"
        "v_exp_f32 v20, v20\n\t"
        "v_exp_f32 v21, v21\n\t"
        "v_exp_f32 v22, v22\n\t"
        "v_exp_f32 v23, v23\n\t"
        "v_smfmac_f32_32x32x32_bf16 a[0:15], a[116:119], v[56:63], v9\n\t"
        "ds_read_b64_tr_b16 a[74:75], v8 offset:21728\n\t"
        "v_exp_f32 v24, v24\n\t"
        "v_exp_f32 v25, v25\n\t"
        "v_exp_f32 v26, v26\n\t"
        "v_exp_f32 v27, v27\n\t"
        "v_smfmac_f32_32x32x32_bf16 a[16:31], a[104:107], v[16:23], v9\n\t"
        "ds_read_b64_tr_b16 a[76:77], v8 offset:22400\n\t"
        "v_exp_f32 v28, v28\n\t"
        "v_exp_f32 v29, v29\n\t"
        "v_exp_f32 v30, v30\n\t"
        "v_exp_f32 v31, v31\n\t"
        "v_mfma_f32_32x32x16_bf16 v[48:63], v[230:233], a[60:63], 0\n\t"
        "ds_read_b64_tr_b16 a[78:79], v8 offset:22624\n\t"
        "v_exp_f32 v32, v32\n\t"
        "v_exp_f32 v33, v33\n\t"
        "v_exp_f32 v34, v34\n\t"
        "v_exp_f32 v35, v35\n\t"
        "v_mfma_f32_32x32x16_bf16 v[48:63], v[226:229], a[56:59], v[48:63]\n\t"
        "ds_read_b64_tr_b16 a[80:81], v8 offset:23296\n\t"
        "v_exp_f32 v36, v36\n\t"
        "v_exp_f32 v37, v37\n\t"
        "v_exp_f32 v38, v38\n\t"
        "v_exp_f32 v39, v39\n\t"
        "v_smfmac_f32_32x32x32_bf16 a[16:31], a[108:111], v[24:31], v9\n\t"
        "v_exp_f32 v40, v40\n\t"
        "v_exp_f32 v41, v41\n\t"
        "v_exp_f32 v42, v42\n\t"
        "v_exp_f32 v43, v43\n\t"
        "v_smfmac_f32_32x32x32_bf16 a[16:31], a[112:115], v[32:39], v9\n\t"
        "v_exp_f32 v44, v44\n\t"
        "v_exp_f32 v45, v45\n\t"
        "v_exp_f32 v46, v46\n\t"
        "v_exp_f32 v47, v47\n\t"
        "v_mfma_f32_32x32x16_bf16 v[16:31], v[238:241], a[60:63], 0\n\t"
        "v_exp_f32 v48, v48\n\t"
        "v_exp_f32 v49, v49\n\t"
        "v_exp_f32 v50, v50\n\t"
        "v_exp_f32 v51, v51\n\t"
        "v_mfma_f32_32x32x16_bf16 v[16:31], v[234:237], a[56:59], v[16:31]\n\t"
        "v_exp_f32 v52, v52\n\t"
        "v_exp_f32 v53, v53\n\t"
        "v_exp_f32 v54, v54\n\t"
        "v_exp_f32 v55, v55\n\t"
        "v_smfmac_f32_32x32x32_bf16 a[16:31], a[116:119], v[40:47], v9\n\t"
        "v_exp_f32 v56, v56\n\t"
        "v_exp_f32 v57, v57\n\t"
        "v_exp_f32 v58, v58\n\t"
        "v_exp_f32 v59, v59\n\t"
        "v_smfmac_f32_32x32x32_bf16 a[32:47], a[104:107], v[48:55], v9\n\t"
        "s_waitcnt vmcnt(4)\n\t"
        "v_exp_f32 v60, v60\n\t"
        "v_exp_f32 v61, v61\n\t"
        "v_exp_f32 v62, v62\n\t"
        "v_exp_f32 v63, v63\n\t"
        "v_mfma_f32_32x32x16_bf16 v[32:47], v[100:103], v[164:167], 0\n\t"
        "ds_read_b64_tr_b16 a[82:83], v8 offset:23520\n\t"
        "v_exp_f32 v16, v16\n\t"
        "v_exp_f32 v17, v17\n\t"
        "v_exp_f32 v18, v18\n\t"
        "v_exp_f32 v19, v19\n\t"
        "v_mfma_f32_32x32x16_bf16 v[32:47], v[96:99], v[160:163], v[32:47]\n\t"
        "ds_read_b64_tr_b16 a[84:85], v8 offset:24192\n\t"
        "v_exp_f32 v20, v20\n\t"
        "v_exp_f32 v21, v21\n\t"
        "v_exp_f32 v22, v22\n\t"
        "v_exp_f32 v23, v23\n\t"
        "v_smfmac_f32_32x32x32_bf16 a[32:47], a[108:111], v[56:63], v9\n\t"
        "ds_read_b64_tr_b16 a[86:87], v8 offset:24416\n\t"
        "v_exp_f32 v24, v24\n\t"
        "v_exp_f32 v25, v25\n\t"
        "v_exp_f32 v26, v26\n\t"
        "v_exp_f32 v27, v27\n\t"
        "v_smfmac_f32_32x32x32_bf16 a[32:47], a[112:115], v[16:23], v9\n\t"
        "v_exp_f32 v28, v28\n\t"
        "v_exp_f32 v29, v29\n\t"
        "v_exp_f32 v30, v30\n\t"
        "v_exp_f32 v31, v31\n\t"
        "v_mfma_f32_32x32x16_bf16 v[48:63], v[108:111], v[164:167], 0\n\t"
        ".L_page2_%=:\n\t"
        "s_cbranch_vccnz .L_touched_%=\n\t"
        "v_exp_f32 v32, v32\n\t"
        "v_exp_f32 v33, v33\n\t"
        "v_exp_f32 v34, v34\n\t"
        "v_exp_f32 v35, v35\n\t"
        "v_mfma_f32_32x32x16_bf16 v[48:63], v[104:107], v[160:163], v[48:63]\n\t"
        "s_waitcnt lgkmcnt(0)\n\t"
        "v_exp_f32 v36, v36\n\t"
        "v_exp_f32 v37, v37\n\t"
        "v_exp_f32 v38, v38\n\t"
        "v_exp_f32 v39, v39\n\t"
        "v_smfmac_f32_32x32x32_bf16 a[32:47], a[116:119], v[24:31], v9\n\t"
        "v_exp_f32 v40, v40\n\t"
        "v_exp_f32 v41, v41\n\t"
        "v_exp_f32 v42, v42\n\t"
        "v_exp_f32 v43, v43\n\t"
        "v_smfmac_f32_32x32x32_bf16 a[0:15], a[72:75], v[32:39], v9\n\t"
        "v_exp_f32 v44, v44\n\t"
        "v_exp_f32 v45, v45\n\t"
        "v_exp_f32 v46, v46\n\t"
        "v_exp_f32 v47, v47\n\t"
        "v_mfma_f32_32x32x16_bf16 v[16:31], v[100:103], a[52:55], 0\n\t"
        "v_exp_f32 v48, v48\n\t"
        "v_exp_f32 v49, v49\n\t"
        "v_exp_f32 v50, v50\n\t"
        "v_exp_f32 v51, v51\n\t"
        "v_mfma_f32_32x32x16_bf16 v[16:31], v[96:99], a[48:51], v[16:31]\n\t"
        "v_exp_f32 v52, v52\n\t"
        "v_exp_f32 v53, v53\n\t"
        "v_exp_f32 v54, v54\n\t"
        "v_exp_f32 v55, v55\n\t"
        "v_smfmac_f32_32x32x32_bf16 a[0:15], a[76:79], v[40:47], v9\n\t"
        "v_exp_f32 v56, v56\n\t"
        "v_exp_f32 v57, v57\n\t"
        "v_exp_f32 v58, v58\n\t"
        "v_exp_f32 v59, v59\n\t"
        "v_smfmac_f32_32x32x32_bf16 a[0:15], a[80:83], v[48:55], v9\n\t"
        "v_exp_f32 v60, v60\n\t"
        "v_exp_f32 v61, v61\n\t"
        "v_exp_f32 v62, v62\n\t"
        "v_exp_f32 v63, v63\n\t"
        "v_mfma_f32_32x32x16_bf16 v[32:47], v[108:111], a[52:55], 0\n\t"
        "s_waitcnt vmcnt(8)\n\t"
        "v_exp_f32 v16, v16\n\t"
        "v_exp_f32 v17, v17\n\t"
        "v_exp_f32 v18, v18\n\t"
        "v_exp_f32 v19, v19\n\t"
        "v_mfma_f32_32x32x16_bf16 v[32:47], v[104:107], a[48:51], v[32:47]\n\t"
        "ds_read_b64_tr_b16 a[88:89], v8 offset:25088\n\t"
        "v_exp_f32 v20, v20\n\t"
        "v_exp_f32 v21, v21\n\t"
        "v_exp_f32 v22, v22\n\t"
        "v_exp_f32 v23, v23\n\t"
        "v_smfmac_f32_32x32x32_bf16 a[0:15], a[84:87], v[56:63], v9\n\t"
        "ds_read_b64_tr_b16 a[90:91], v8 offset:25312\n\t"
        "v_exp_f32 v24, v24\n\t"
        "v_exp_f32 v25, v25\n\t"
        "v_exp_f32 v26, v26\n\t"
        "v_exp_f32 v27, v27\n\t"
        "v_smfmac_f32_32x32x32_bf16 a[16:31], a[72:75], v[16:23], v9\n\t"
        "ds_read_b64_tr_b16 a[92:93], v8 offset:25984\n\t"
        "v_exp_f32 v28, v28\n\t"
        "v_exp_f32 v29, v29\n\t"
        "v_exp_f32 v30, v30\n\t"
        "v_exp_f32 v31, v31\n\t"
        "v_mfma_f32_32x32x16_bf16 v[48:63], v[100:103], a[60:63], 0\n\t"
        "ds_read_b64_tr_b16 a[94:95], v8 offset:26208\n\t"
        "v_exp_f32 v32, v32\n\t"
        "v_exp_f32 v33, v33\n\t"
        "v_exp_f32 v34, v34\n\t"
        "v_exp_f32 v35, v35\n\t"
        "v_mfma_f32_32x32x16_bf16 v[48:63], v[96:99], a[56:59], v[48:63]\n\t"
        "ds_read_b64_tr_b16 a[96:97], v8 offset:26880\n\t"
        "v_exp_f32 v36, v36\n\t"
        "v_exp_f32 v37, v37\n\t"
        "v_exp_f32 v38, v38\n\t"
        "v_exp_f32 v39, v39\n\t"
        "v_smfmac_f32_32x32x32_bf16 a[16:31], a[76:79], v[24:31], v9\n\t"
        "v_exp_f32 v40, v40\n\t"
        "v_exp_f32 v41, v41\n\t"
        "v_exp_f32 v42, v42\n\t"
        "v_exp_f32 v43, v43\n\t"
        "v_smfmac_f32_32x32x32_bf16 a[16:31], a[80:83], v[32:39], v9\n\t"
        "v_exp_f32 v44, v44\n\t"
        "v_exp_f32 v45, v45\n\t"
        "v_exp_f32 v46, v46\n\t"
        "v_exp_f32 v47, v47\n\t"
        "v_mfma_f32_32x32x16_bf16 v[16:31], v[108:111], a[60:63], 0\n\t"
        "v_exp_f32 v48, v48\n\t"
        "v_exp_f32 v49, v49\n\t"
        "v_exp_f32 v50, v50\n\t"
        "v_exp_f32 v51, v51\n\t"
        "v_mfma_f32_32x32x16_bf16 v[16:31], v[104:107], a[56:59], v[16:31]\n\t"
        "v_exp_f32 v52, v52\n\t"
        "v_exp_f32 v53, v53\n\t"
        "v_exp_f32 v54, v54\n\t"
        "v_exp_f32 v55, v55\n\t"
        "v_smfmac_f32_32x32x32_bf16 a[16:31], a[84:87], v[40:47], v9\n\t"
        "v_exp_f32 v56, v56\n\t"
        "v_exp_f32 v57, v57\n\t"
        "v_exp_f32 v58, v58\n\t"
        "v_exp_f32 v59, v59\n\t"
        "v_smfmac_f32_32x32x32_bf16 a[32:47], a[72:75], v[48:55], v9\n\t"
        "s_waitcnt vmcnt(0)\n\t"
        "v_exp_f32 v60, v60\n\t"
        "v_exp_f32 v61, v61\n\t"
        "v_exp_f32 v62, v62\n\t"
        "v_exp_f32 v63, v63\n\t"
        "v_mfma_f32_32x32x16_bf16 v[32:47], v[116:119], v[164:167], 0\n\t"
        "ds_read_b64_tr_b16 a[98:99], v8 offset:27104\n\t"
        "v_exp_f32 v16, v16\n\t"
        "v_exp_f32 v17, v17\n\t"
        "v_exp_f32 v18, v18\n\t"
        "v_exp_f32 v19, v19\n\t"
        "v_mfma_f32_32x32x16_bf16 v[32:47], v[112:115], v[160:163], v[32:47]\n\t"
        "ds_read_b64_tr_b16 a[100:101], v8 offset:27776\n\t"
        "v_exp_f32 v20, v20\n\t"
        "v_exp_f32 v21, v21\n\t"
        "v_exp_f32 v22, v22\n\t"
        "v_exp_f32 v23, v23\n\t"
        "v_smfmac_f32_32x32x32_bf16 a[32:47], a[76:79], v[56:63], v9\n\t"
        "ds_read_b64_tr_b16 a[102:103], v8 offset:28000\n\t"
        "v_exp_f32 v24, v24\n\t"
        "v_exp_f32 v25, v25\n\t"
        "v_exp_f32 v26, v26\n\t"
        "v_exp_f32 v27, v27\n\t"
        "v_smfmac_f32_32x32x32_bf16 a[32:47], a[80:83], v[16:23], v9\n\t"
        "v_exp_f32 v28, v28\n\t"
        "v_exp_f32 v29, v29\n\t"
        "v_exp_f32 v30, v30\n\t"
        "v_exp_f32 v31, v31\n\t"
        "v_mfma_f32_32x32x16_bf16 v[48:63], v[124:127], v[164:167], 0\n\t"
        "v_exp_f32 v32, v32\n\t"
        "v_exp_f32 v33, v33\n\t"
        "v_exp_f32 v34, v34\n\t"
        "v_exp_f32 v35, v35\n\t"
        "v_mfma_f32_32x32x16_bf16 v[48:63], v[120:123], v[160:163], v[48:63]\n\t"
        "s_waitcnt lgkmcnt(0)\n\t"
        "v_exp_f32 v36, v36\n\t"
        "v_exp_f32 v37, v37\n\t"
        "v_exp_f32 v38, v38\n\t"
        "v_exp_f32 v39, v39\n\t"
        "v_smfmac_f32_32x32x32_bf16 a[32:47], a[84:87], v[24:31], v9\n\t"
        "v_exp_f32 v40, v40\n\t"
        "v_exp_f32 v41, v41\n\t"
        "v_exp_f32 v42, v42\n\t"
        "v_exp_f32 v43, v43\n\t"
        "v_smfmac_f32_32x32x32_bf16 a[0:15], a[88:91], v[32:39], v9\n\t"
        "v_exp_f32 v44, v44\n\t"
        "v_exp_f32 v45, v45\n\t"
        "v_exp_f32 v46, v46\n\t"
        "v_exp_f32 v47, v47\n\t"
        "v_mfma_f32_32x32x16_bf16 v[16:31], v[116:119], a[52:55], 0\n\t"
        "v_exp_f32 v48, v48\n\t"
        "v_exp_f32 v49, v49\n\t"
        "v_exp_f32 v50, v50\n\t"
        "v_exp_f32 v51, v51\n\t"
        "v_mfma_f32_32x32x16_bf16 v[16:31], v[112:115], a[48:51], v[16:31]\n\t"
        "v_exp_f32 v52, v52\n\t"
        "v_exp_f32 v53, v53\n\t"
        "v_exp_f32 v54, v54\n\t"
        "v_exp_f32 v55, v55\n\t"
        "v_smfmac_f32_32x32x32_bf16 a[0:15], a[92:95], v[40:47], v9\n\t"
        "v_exp_f32 v56, v56\n\t"
        "v_exp_f32 v57, v57\n\t"
        "v_exp_f32 v58, v58\n\t"
        "v_exp_f32 v59, v59\n\t"
        "v_smfmac_f32_32x32x32_bf16 a[0:15], a[96:99], v[48:55], v9\n\t"
        "v_exp_f32 v60, v60\n\t"
        "v_exp_f32 v61, v61\n\t"
        "v_exp_f32 v62, v62\n\t"
        "v_exp_f32 v63, v63\n\t"
        "v_mfma_f32_32x32x16_bf16 v[32:47], v[124:127], a[52:55], 0\n\t"
        "v_exp_f32 v16, v16\n\t"
        "v_exp_f32 v17, v17\n\t"
        "v_exp_f32 v18, v18\n\t"
        "v_exp_f32 v19, v19\n\t"
        "v_mfma_f32_32x32x16_bf16 v[32:47], v[120:123], a[48:51], v[32:47]\n\t"
        "v_exp_f32 v20, v20\n\t"
        "v_exp_f32 v21, v21\n\t"
        "v_exp_f32 v22, v22\n\t"
        "v_exp_f32 v23, v23\n\t"
        "v_smfmac_f32_32x32x32_bf16 a[0:15], a[100:103], v[56:63], v9\n\t"
        "v_exp_f32 v24, v24\n\t"
        "v_exp_f32 v25, v25\n\t"
        "v_exp_f32 v26, v26\n\t"
        "v_exp_f32 v27, v27\n\t"
        "v_smfmac_f32_32x32x32_bf16 a[16:31], a[88:91], v[16:23], v9\n\t"
        "v_exp_f32 v28, v28\n\t"
        "v_exp_f32 v29, v29\n\t"
        "v_exp_f32 v30, v30\n\t"
        "v_exp_f32 v31, v31\n\t"
        "v_mfma_f32_32x32x16_bf16 v[48:63], v[116:119], a[60:63], 0\n\t"
        "v_exp_f32 v32, v32\n\t"
        "v_exp_f32 v33, v33\n\t"
        "v_exp_f32 v34, v34\n\t"
        "v_exp_f32 v35, v35\n\t"
        "v_mfma_f32_32x32x16_bf16 v[48:63], v[112:115], a[56:59], v[48:63]\n\t"
        "ds_write_b128 v216, a[0:3] offset:0\n\t"
        "v_exp_f32 v36, v36\n\t"
        "v_exp_f32 v37, v37\n\t"
        "v_exp_f32 v38, v38\n\t"
        "v_exp_f32 v39, v39\n\t"
        "v_smfmac_f32_32x32x32_bf16 a[16:31], a[92:95], v[24:31], v9\n\t"
        "ds_write_b128 v216, a[4:7] offset:1024\n\t"
        "v_exp_f32 v40, v40\n\t"
        "v_exp_f32 v41, v41\n\t"
        "v_exp_f32 v42, v42\n\t"
        "v_exp_f32 v43, v43\n\t"
        "v_smfmac_f32_32x32x32_bf16 a[16:31], a[96:99], v[32:39], v9\n\t"
        "ds_write_b128 v216, a[8:11] offset:2048\n\t"
        "v_exp_f32 v44, v44\n\t"
        "v_exp_f32 v45, v45\n\t"
        "v_exp_f32 v46, v46\n\t"
        "v_exp_f32 v47, v47\n\t"
        "v_mfma_f32_32x32x16_bf16 v[16:31], v[124:127], a[60:63], 0\n\t"
        "ds_write_b128 v216, a[12:15] offset:3072\n\t"
        "v_exp_f32 v48, v48\n\t"
        "v_exp_f32 v49, v49\n\t"
        "v_exp_f32 v50, v50\n\t"
        "v_exp_f32 v51, v51\n\t"
        "v_mfma_f32_32x32x16_bf16 v[16:31], v[120:123], a[56:59], v[16:31]\n\t"
        "v_exp_f32 v52, v52\n\t"
        "v_exp_f32 v53, v53\n\t"
        "v_exp_f32 v54, v54\n\t"
        "v_exp_f32 v55, v55\n\t"
        "v_smfmac_f32_32x32x32_bf16 a[16:31], a[100:103], v[40:47], v9\n\t"
        "v_exp_f32 v56, v56\n\t"
        "v_exp_f32 v57, v57\n\t"
        "v_exp_f32 v58, v58\n\t"
        "v_exp_f32 v59, v59\n\t"
        "v_smfmac_f32_32x32x32_bf16 a[32:47], a[88:91], v[48:55], v9\n\t"
        "v_exp_f32 v60, v60\n\t"
        "v_exp_f32 v61, v61\n\t"
        "v_exp_f32 v62, v62\n\t"
        "v_exp_f32 v63, v63\n\t"
        "v_mfma_f32_32x32x16_bf16 v[32:47], v[230:233], a[68:71], 0\n\t"
        "v_exp_f32 v16, v16\n\t"
        "v_exp_f32 v17, v17\n\t"
        "v_exp_f32 v18, v18\n\t"
        "v_exp_f32 v19, v19\n\t"
        "v_mfma_f32_32x32x16_bf16 v[32:47], v[226:229], a[64:67], v[32:47]\n\t"
        "ds_write_b128 v216, a[16:19] offset:4096\n\t"
        "v_exp_f32 v20, v20\n\t"
        "v_exp_f32 v21, v21\n\t"
        "v_exp_f32 v22, v22\n\t"
        "v_exp_f32 v23, v23\n\t"
        "v_smfmac_f32_32x32x32_bf16 a[32:47], a[92:95], v[56:63], v9\n\t"
        "ds_write_b128 v216, a[20:23] offset:5120\n\t"
        "v_exp_f32 v24, v24\n\t"
        "v_exp_f32 v25, v25\n\t"
        "v_exp_f32 v26, v26\n\t"
        "v_exp_f32 v27, v27\n\t"
        "v_smfmac_f32_32x32x32_bf16 a[32:47], a[96:99], v[16:23], v9\n\t"
        "ds_write_b128 v216, a[24:27] offset:6144\n\t"
        "v_exp_f32 v28, v28\n\t"
        "v_exp_f32 v29, v29\n\t"
        "v_exp_f32 v30, v30\n\t"
        "v_exp_f32 v31, v31\n\t"
        "v_mfma_f32_32x32x16_bf16 v[48:63], v[238:241], a[68:71], 0\n\t"
        "ds_write_b128 v216, a[28:31] offset:7168\n\t"
        "v_exp_f32 v32, v32\n\t"
        "v_exp_f32 v33, v33\n\t"
        "v_exp_f32 v34, v34\n\t"
        "v_exp_f32 v35, v35\n\t"
        "v_mfma_f32_32x32x16_bf16 v[48:63], v[234:237], a[64:67], v[48:63]\n\t"
        "v_exp_f32 v36, v36\n\t"
        "v_exp_f32 v37, v37\n\t"
        "v_exp_f32 v38, v38\n\t"
        "v_exp_f32 v39, v39\n\t"
        "v_smfmac_f32_32x32x32_bf16 a[32:47], a[100:103], v[24:31], v9\n\t"
        "v_exp_f32 v40, v40\n\t"
        "v_exp_f32 v41, v41\n\t"
        "v_exp_f32 v42, v42\n\t"
        "v_exp_f32 v43, v43\n\t"
        "v_smfmac_f32_32x32x32_bf16 v[144:159], a[104:107], v[32:39], v9\n\t"
        "v_exp_f32 v44, v44\n\t"
        "v_exp_f32 v45, v45\n\t"
        "v_exp_f32 v46, v46\n\t"
        "v_exp_f32 v47, v47\n\t"
        "v_mfma_f32_32x32x16_bf16 v[16:31], v[100:103], a[68:71], 0\n\t"
        "v_exp_f32 v48, v48\n\t"
        "v_exp_f32 v49, v49\n\t"
        "v_exp_f32 v50, v50\n\t"
        "v_exp_f32 v51, v51\n\t"
        "v_mfma_f32_32x32x16_bf16 v[16:31], v[96:99], a[64:67], v[16:31]\n\t"
        "ds_write_b128 v216, a[32:35] offset:8192\n\t"
        "v_exp_f32 v52, v52\n\t"
        "v_exp_f32 v53, v53\n\t"
        "v_exp_f32 v54, v54\n\t"
        "v_exp_f32 v55, v55\n\t"
        "v_smfmac_f32_32x32x32_bf16 v[144:159], a[108:111], v[40:47], v9\n\t"
        "ds_write_b128 v216, a[36:39] offset:9216\n\t"
        "v_exp_f32 v56, v56\n\t"
        "v_exp_f32 v57, v57\n\t"
        "v_exp_f32 v58, v58\n\t"
        "v_exp_f32 v59, v59\n\t"
        "v_smfmac_f32_32x32x32_bf16 v[144:159], a[112:115], v[48:55], v9\n\t"
        "ds_write_b128 v216, a[40:43] offset:10240\n\t"
        "v_exp_f32 v60, v60\n\t"
        "v_exp_f32 v61, v61\n\t"
        "v_exp_f32 v62, v62\n\t"
        "v_exp_f32 v63, v63\n\t"
        "v_mfma_f32_32x32x16_bf16 v[32:47], v[108:111], a[68:71], 0\n\t"
        "ds_write_b128 v216, a[44:47] offset:11264\n\t"
        "v_exp_f32 v16, v16\n\t"
        "v_exp_f32 v17, v17\n\t"
        "v_exp_f32 v18, v18\n\t"
        "v_exp_f32 v19, v19\n\t"
        "v_mfma_f32_32x32x16_bf16 v[32:47], v[104:107], a[64:67], v[32:47]\n\t"
        "v_exp_f32 v20, v20\n\t"
        "v_exp_f32 v21, v21\n\t"
        "v_exp_f32 v22, v22\n\t"
        "v_exp_f32 v23, v23\n\t"
        "v_smfmac_f32_32x32x32_bf16 v[144:159], a[116:119], v[56:63], v9\n\t"
        "v_exp_f32 v24, v24\n\t"
        "v_exp_f32 v25, v25\n\t"
        "v_exp_f32 v26, v26\n\t"
        "v_exp_f32 v27, v27\n\t"
        "v_smfmac_f32_32x32x32_bf16 v[144:159], a[72:75], v[16:23], v9\n\t"
        "v_exp_f32 v28, v28\n\t"
        "v_exp_f32 v29, v29\n\t"
        "v_exp_f32 v30, v30\n\t"
        "v_exp_f32 v31, v31\n\t"
        "v_mfma_f32_32x32x16_bf16 v[48:63], v[116:119], a[68:71], 0\n\t"
        "s_waitcnt lgkmcnt(0)\n\t"
        "v_exp_f32 v32, v32\n\t"
        "v_exp_f32 v33, v33\n\t"
        "v_exp_f32 v34, v34\n\t"
        "v_exp_f32 v35, v35\n\t"
        "v_mfma_f32_32x32x16_bf16 v[48:63], v[112:115], a[64:67], v[48:63]\n\t"
        "s_barrier\n\t"
        "ds_read_b128 v[64:67], v213 offset:0\n\t"
        "ds_read_b128 v[80:83], v214 offset:4096\n\t"
        "v_exp_f32 v36, v36\n\t"
        "v_exp_f32 v37, v37\n\t"
        "v_exp_f32 v38, v38\n\t"
        "v_exp_f32 v39, v39\n\t"
        "v_smfmac_f32_32x32x32_bf16 v[144:159], a[76:79], v[24:31], v9\n\t"
        "ds_read_b128 v[68:71], v213 offset:1024\n\t"
        "ds_read_b128 v[84:87], v214 offset:5120\n\t"
        "v_exp_f32 v40, v40\n\t"
        "v_exp_f32 v41, v41\n\t"
        "v_exp_f32 v42, v42\n\t"
        "v_exp_f32 v43, v43\n\t"
        "v_smfmac_f32_32x32x32_bf16 v[144:159], a[80:83], v[32:39], v9\n\t"
        "ds_read_b128 v[72:75], v213 offset:2048\n\t"
        "ds_read_b128 v[88:91], v214 offset:6144\n\t"
        "v_exp_f32 v44, v44\n\t"
        "v_exp_f32 v45, v45\n\t"
        "v_exp_f32 v46, v46\n\t"
        "v_exp_f32 v47, v47\n\t"
        "v_mfma_f32_32x32x16_bf16 v[16:31], v[124:127], a[68:71], 0\n\t"
        "ds_read_b128 v[76:79], v213 offset:3072\n\t"
        "ds_read_b128 v[92:95], v214 offset:7168\n\t"
        "v_exp_f32 v48, v48\n\t"
        "v_exp_f32 v49, v49\n\t"
        "v_exp_f32 v50, v50\n\t"
        "v_exp_f32 v51, v51\n\t"
        "v_mfma_f32_32x32x16_bf16 v[16:31], v[120:123], a[64:67], v[16:31]\n\t"
        "ds_read_b128 v[180:183], v215 offset:11264\n\t"
        "ds_read_b128 v[168:171], v215 offset:8192\n\t"
        "v_exp_f32 v52, v52\n\t"
        "v_exp_f32 v53, v53\n\t"
        "v_exp_f32 v54, v54\n\t"
        "v_exp_f32 v55, v55\n\t"
        "v_smfmac_f32_32x32x32_bf16 v[144:159], a[84:87], v[40:47], v9\n\t"
        "ds_read_b128 v[172:175], v215 offset:9216\n\t"
        "ds_read_b128 v[176:179], v215 offset:10240\n\t"
        "v_exp_f32 v56, v56\n\t"
        "v_exp_f32 v57, v57\n\t"
        "v_exp_f32 v58, v58\n\t"
        "v_exp_f32 v59, v59\n\t"
        "v_smfmac_f32_32x32x32_bf16 v[144:159], a[88:91], v[48:55], v9\n\t"
        "s_waitcnt lgkmcnt(4)\n\t"
        "v_exp_f32 v60, v60\n\t"
        "v_exp_f32 v61, v61\n\t"
        "v_exp_f32 v62, v62\n\t"
        "v_exp_f32 v63, v63\n\t"
        "v_pk_add_f32 v[64:65], v[64:65], v[80:81]\n\t"
        "v_pk_add_f32 v[66:67], v[66:67], v[82:83]\n\t"
        "v_pk_add_f32 v[68:69], v[68:69], v[84:85]\n\t"
        "v_pk_add_f32 v[70:71], v[70:71], v[86:87]\n\t"
        "v_smfmac_f32_32x32x32_bf16 v[144:159], a[92:95], v[56:63], v9\n\t"
        "v_exp_f32 v16, v16\n\t"
        "v_exp_f32 v17, v17\n\t"
        "v_exp_f32 v18, v18\n\t"
        "v_exp_f32 v19, v19\n\t"
        "v_exp_f32 v20, v20\n\t"
        "v_exp_f32 v21, v21\n\t"
        "v_exp_f32 v22, v22\n\t"
        "v_exp_f32 v23, v23\n\t"
        "v_exp_f32 v24, v24\n\t"
        "v_exp_f32 v25, v25\n\t"
        "v_pk_add_f32 v[72:73], v[72:73], v[88:89]\n\t"
        "v_pk_add_f32 v[74:75], v[74:75], v[90:91]\n\t"
        "v_pk_add_f32 v[76:77], v[76:77], v[92:93]\n\t"
        "v_pk_add_f32 v[78:79], v[78:79], v[94:95]\n\t"
        "v_smfmac_f32_32x32x32_bf16 v[144:159], a[96:99], v[16:23], v9\n\t"
        "s_waitcnt lgkmcnt(0)\n\t"
        "v_exp_f32 v26, v26\n\t"
        "v_exp_f32 v27, v27\n\t"
        "v_exp_f32 v28, v28\n\t"
        "v_exp_f32 v29, v29\n\t"
        "v_exp_f32 v30, v30\n\t"
        "v_exp_f32 v31, v31\n\t"
        "v_add_f32 v76, v76, v180\n\t"
        "v_add_f32 v77, v77, v181\n\t"
        "v_smfmac_f32_32x32x32_bf16 v[144:159], a[100:103], v[24:31], v9\n\t"
        "v_add_f32 v64, v64, v168\n\t"
        "v_add_f32 v65, v65, v169\n\t"
        "v_add_f32 v66, v66, v170\n\t"
        "v_add_f32 v67, v67, v171\n\t"
        "v_add_f32 v68, v68, v172\n\t"
        "v_add_f32 v69, v69, v173\n\t"
        "v_add_f32 v70, v70, v174\n\t"
        "v_add_f32 v71, v71, v175\n\t"
        "v_add_f32 v72, v72, v176\n\t"
        "v_add_f32 v73, v73, v177\n\t"
        "v_add_f32 v74, v74, v178\n\t"
        "v_add_f32 v75, v75, v179\n\t"
        "v_add_f32 v78, v78, v182\n\t"
        "v_add_f32 v79, v79, v183\n\t"
        "v_pk_add_f32 v[156:157], v[156:157], v[76:77]\n\t"
        "v_rcp_f32 v12, v156\n\t"
        "v_pk_add_f32 v[158:159], v[158:159], v[78:79]\n\t"
        "v_mov_b32 v13, v12\n\t"
        "v_pk_add_f32 v[152:153], v[152:153], v[72:73]\n\t"
        "v_pk_add_f32 v[154:155], v[154:155], v[74:75]\n\t"
        "v_permlane32_swap_b32 v12, v13\n\t"
        "v_pk_add_f32 v[144:145], v[144:145], v[64:65]\n\t"
        "v_pk_mul_f32 v[152:153], v[152:153], v[12:13] op_sel:[0,1] op_sel_hi:[1,1]\n\t"
        "v_pk_mul_f32 v[154:155], v[154:155], v[12:13] op_sel:[0,1] op_sel_hi:[1,1]\n\t"
        "v_pk_mul_f32 v[156:157], v[156:157], v[12:13] op_sel:[0,1] op_sel_hi:[1,1]\n\t"
        "v_pk_mul_f32 v[158:159], v[158:159], v[12:13] op_sel:[0,1] op_sel_hi:[1,1]\n\t"
        "v_cvt_pk_bf16_f32 v84, v152, v153\n\t"
        "v_cvt_pk_bf16_f32 v85, v154, v155\n\t"
        "v_cvt_pk_bf16_f32 v86, v156, v157\n\t"
        "v_cvt_pk_bf16_f32 v87, v158, v159\n\t"
        "ds_write2_b64 v217, v[84:85], v[86:87] offset0:2 offset1:3\n\t"
        "v_pk_add_f32 v[146:147], v[146:147], v[66:67]\n\t"
        "v_pk_add_f32 v[148:149], v[148:149], v[68:69]\n\t"
        "v_pk_add_f32 v[150:151], v[150:151], v[70:71]\n\t"
        "v_pk_mul_f32 v[144:145], v[144:145], v[12:13] op_sel:[0,1] op_sel_hi:[1,1]\n\t"
        "v_pk_mul_f32 v[146:147], v[146:147], v[12:13] op_sel:[0,1] op_sel_hi:[1,1]\n\t"
        "v_pk_mul_f32 v[148:149], v[148:149], v[12:13] op_sel:[0,1] op_sel_hi:[1,1]\n\t"
        "v_pk_mul_f32 v[150:151], v[150:151], v[12:13] op_sel:[0,1] op_sel_hi:[1,1]\n\t"
        "v_cvt_pk_bf16_f32 v80, v144, v145\n\t"
        "v_cvt_pk_bf16_f32 v81, v146, v147\n\t"
        "v_cvt_pk_bf16_f32 v82, v148, v149\n\t"
        "v_cvt_pk_bf16_f32 v83, v150, v151\n\t"
        "ds_write2_b64 v217, v[80:81], v[82:83] offset0:0 offset1:1\n\t"
        "ds_read_b128 v[64:67], v216 offset:12288\n\t"
        "ds_read_b128 v[68:71], v216 offset:13312\n\t"
        "s_waitcnt lgkmcnt(1)\n\t"
        "buffer_store_dwordx4 v[64:67], v15, s[16:19], s43 offen sc0 sc1\n\t"
        "s_mov_b32 exec_hi, 0xffff\n\t"
        "s_waitcnt lgkmcnt(0)\n\t"
        "buffer_store_dwordx4 v[68:71], v15, s[16:19], s43 offen offset:1024 sc0 sc1\n\t"
        "s_endpgm\n\t"
        ".L_self_%=:\n\t"
        "s_lshl2_add_u32 s56, %1, s40\n\t"
        "s_lshl_b32 s56, s56, 15\n\t"
        "s_add_u32 s56, s56, 4194304\n\t"
        "s_add_u32 s4, s52, s56\n\t"
        "s_addc_u32 s5, s53, 0\n\t"
        "s_mov_b32 s57, s56\n\t"
        "s_mov_b32 s58, 10\n\t"
        "v_mov_b32 v175, 0x3f00\n\t"
        ".L_own_%=:\n\t"
        "buffer_load_dwordx4 v[242:245], v210, s[24:27], s50 offen\n\t"
        "buffer_load_dwordx3 v[246:248], v211, s[24:27], s50 offen offset:32\n\t"
        "v_mov_b32 v224, 0\n\t"
        "s_waitcnt vmcnt(0)\n\t"
        "v_dot2c_f32_bf16 v224, v242, v242\n\t"
        "v_dot2c_f32_bf16 v224, v243, v243\n\t"
        "v_dot2c_f32_bf16 v224, v244, v244\n\t"
        "v_dot2c_f32_bf16 v224, v245, v245\n\t"
        "v_dot2c_f32_bf16 v224, v246, v246\n\t"
        "v_dot2c_f32_bf16 v224, v247, v247\n\t"
        "v_dot2c_f32_bf16 v224, v248, v248\n\t"
        "v_lshlrev_b32 v182, 16, v242\n\t"
        "v_and_b32 v183, s13, v242\n\t"
        "v_lshlrev_b32 v184, 16, v243\n\t"
        "v_and_b32 v185, s13, v243\n\t"
        "v_mov_b32 v225, v224\n\t"
        "v_lshlrev_b32 v186, 16, v244\n\t"
        "v_and_b32 v187, s13, v244\n\t"
        "v_permlane32_swap_b32 v224, v225\n\t"
        "v_lshlrev_b32 v188, 16, v245\n\t"
        "v_add_f32 v224, v224, v225\n\t"
        "v_sqrt_f32 v224, v224\n\t"
        "v_and_b32 v189, s13, v245\n\t"
        "v_add_f32 v224, s45, v224\n\t"
        "v_rcp_f32 v250, v224\n\t"
        "v_lshlrev_b32 v190, 16, v246\n\t"
        "v_and_b32 v191, s13, v246\n\t"
        "v_lshlrev_b32 v192, 16, v247\n\t"
        "v_and_b32 v193, s13, v247\n\t"
        "v_lshlrev_b32 v194, 16, v248\n\t"
        "v_and_b32 v195, s13, v248\n\t"
        "v_pk_mul_f32 v[182:183], v[182:183], v[250:251] op_sel_hi:[1,0]\n\t"
        "v_pk_mul_f32 v[184:185], v[184:185], v[250:251] op_sel_hi:[1,0]\n\t"
        "v_pk_mul_f32 v[186:187], v[186:187], v[250:251] op_sel_hi:[1,0]\n\t"
        "v_pk_mul_f32 v[188:189], v[188:189], v[250:251] op_sel_hi:[1,0]\n\t"
        "v_pk_mul_f32 v[190:191], v[190:191], v[250:251] op_sel_hi:[1,0]\n\t"
        "v_pk_mul_f32 v[192:193], v[192:193], v[250:251] op_sel_hi:[1,0]\n\t"
        "v_pk_mul_f32 v[194:195], v[194:195], v[250:251] op_sel_hi:[1,0]\n\t"
        "v_cvt_pk_bf16_f32 v168, v182, v183\n\t"
        "v_cvt_pk_bf16_f32 v169, v184, v185\n\t"
        "v_cvt_pk_bf16_f32 v170, v186, v187\n\t"
        "v_cvt_pk_bf16_f32 v171, v188, v189\n\t"
        "v_cvt_pk_bf16_f32 v172, v190, v191\n\t"
        "v_cvt_pk_bf16_f32 v173, v192, v193\n\t"
        "v_cvt_pk_bf16_f32 v174, v194, v195\n\t"
        "buffer_store_dwordx4 v[168:171], v15, s[52:55], s57 offen offset:0 sc1\n\t"
        "buffer_store_dwordx4 v[172:175], v15, s[52:55], s57 offen offset:1024 sc1\n\t"
        "s_addk_i32 s50, 1792\n\t"
        "s_addk_i32 s57, 2048\n\t"
        "s_sub_u32 s58, s58, 1\n\t"
        "s_cmp_lg_u32 s58, 0\n\t"
        "s_cbranch_scc1 .L_own_%=\n\t"
        "s_waitcnt vmcnt(0)\n\t"
        "s_mov_b64 vcc, 0\n\t"
        "s_branch .L_go_%=\n\t"
        ".L_general_%=:\n\t"
        "s_waitcnt vmcnt(0) lgkmcnt(0)\n\t"
        "s_mov_b32 m0, s49\n\t"
        "s_nop 15\n\t"
        : "+s"(ka)
        : "s"(blockIdx.x), "v"(threadIdx.x), "s"((unsigned)(size_t)lds)
        :
        "v2", "v3", "v4", "v5", "v6", "v7", "v8", "v9", "v10", "v11", "v12", "v13", "v14", "v15", "v16", "v17", "v18", "v19", "v20", "v21", "v22", "v23", "v24", "v25",
        "v26", "v27", "v28", "v29", "v30", "v31", "v32", "v33", "v34", "v35", "v36", "v37", "v38", "v39", "v40", "v41", "v42", "v43", "v44", "v45", "v46", "v47", "v48", "v49",
        "v50", "v51", "v52", "v53", "v54", "v55", "v56", "v57", "v58", "v59", "v60", "v61", "v62", "v63", "v64", "v65", "v66", "v67", "v68", "v69", "v70", "v71", "v72", "v73",
        "v74", "v75", "v76", "v77", "v78", "v79", "v80", "v81", "v82", "v83", "v84", "v85", "v86", "v87", "v88", "v89", "v90", "v91", "v92", "v93", "v94", "v95", "v96", "v97",
        "v98", "v99", "v100", "v101", "v102", "v103", "v104", "v105", "v106", "v107", "v108", "v109", "v110", "v111", "v112", "v113", "v114", "v115", "v116", "v117", "v118", "v119", "v120", "v121",
        "v122", "v123", "v124", "v125", "v126", "v127", "v128", "v129", "v130", "v131", "v132", "v133", "v134", "v135", "v136", "v137", "v138", "v139", "v140", "v141", "v142", "v143", "v144", "v145",
        "v146", "v147", "v148", "v149", "v150", "v151", "v152", "v153", "v154", "v155", "v156", "v157", "v158", "v159", "v160", "v161", "v162", "v163", "v164", "v165", "v166", "v167", "v168", "v169",
        "v170", "v171", "v172", "v173", "v174", "v175", "v176", "v177", "v178", "v179", "v180", "v181", "v182", "v183", "v184", "v185", "v186", "v187", "v188", "v189", "v190", "v191", "v192", "v193",
        "v194", "v195", "v196", "v197", "v198", "v199", "v200", "v201", "v202", "v203", "v204", "v205", "v206", "v207", "v208", "v209", "v210", "v211", "v212", "v213", "v214", "v215", "v216", "v217",
        "v218", "v219", "v220", "v221", "v222", "v223", "v224", "v225", "v226", "v227", "v228", "v229", "v230", "v231", "v232", "v233", "v234", "v235", "v236", "v237", "v238", "v239", "v240", "v241",
        "v242", "v243", "v244", "v245", "v246", "v247", "v248", "v249", "v250", "v251", "v252", "a0", "a1", "a2", "a3", "a4", "a5", "a6", "a7", "a8", "a9", "a10", "a11", "a12",
        "a13", "a14", "a15", "a16", "a17", "a18", "a19", "a20", "a21", "a22", "a23", "a24", "a25", "a26", "a27", "a28", "a29", "a30", "a31", "a32", "a33", "a34", "a35", "a36",
        "a37", "a38", "a39", "a40", "a41", "a42", "a43", "a44", "a45", "a46", "a47", "a48", "a49", "a50", "a51", "a52", "a53", "a54", "a55", "a56", "a57", "a58", "a59", "a60",
        "a61", "a62", "a63", "a64", "a65", "a66", "a67", "a68", "a69", "a70", "a71", "a72", "a73", "a74", "a75", "a76", "a77", "a78", "a79", "a80", "a81", "a82", "a83", "a84",
        "a85", "a86", "a87", "a88", "a89", "a90", "a91", "a92", "a93", "a94", "a95", "a96", "a97", "a98", "a99", "a100", "a101", "a102", "a103", "a104", "a105", "a106", "a107", "a108",
        "a109", "a110", "a111", "a112", "a113", "a114", "a115", "a116", "a117", "a118", "a119", "a120", "a121", "a122", "a123", "a124", "a125", "a126", "a127", "s4", "s5", "s6", "s7", "s8",
        "s9", "s10", "s11", "s12", "s13", "s14", "s15", "s16", "s17", "s18", "s19", "s20", "s21", "s22", "s23", "s24", "s25", "s26", "s27", "s28", "s29", "s30", "s31", "s36",
        "s37", "s38", "s39", "s40", "s41", "s42", "s43", "s44", "s45", "s46", "s47", "s48", "s49", "s50", "s51", "s52", "s53", "s54", "s55", "s56", "s57", "s58", "s59", "vcc",
        "scc", "memory");
    const int b = blockIdx.x, head = (b & 7) * 2 + (b >> 7), qblk = (b >> 3) & 15;
    u16* const* args = (u16* const*)ka;
    general_path(args[0], args[1], args[2], bf2f(args[3][head]), args[4], head, qblk);
}

extern "C" void kernel_launch(void* const* d_in, const int* in_sizes, int n_in, void* d_out, int out_size, void* d_ws, size_t ws_size, hipStream_t stream) {
    (void)in_sizes; (void)n_in; (void)out_size; (void)ws_size;
    qk_norm_attn_kernel<<<256, 256, 0, stream>>>((const u16*)d_in[0], (const u16*)d_in[1], (const u16*)d_in[2], (const u16*)d_in[3], (u16*)d_out, d_ws);
}
```
